```python
import torch

def _softmax(x, axis=-1):
    return torch.softmax(x, dim=axis)
def _gelu(x):
    return torch.nn.functional.gelu(x, approximate="tanh")
def _silu(x):
    return torch.nn.functional.silu(x)
def _sigmoid(x):
    return torch.sigmoid(x)
def _layernorm(x, axis=-1, eps=1e-5):
    m = x.mean(dim=axis, keepdim=True)
    v = x.var(dim=axis, keepdim=True, unbiased=False)
    return (x - m) / torch.sqrt(v + eps)
def _scatter_add(tgt, idx, src, axis=0):
    return tgt.clone().index_add_(axis, idx, src)
def _segment_sum(src, seg, n):
    out = torch.zeros((n,) + src.shape[1:], dtype=src.dtype)
    return out.index_add_(0, seg, src)
def _pad(x, pw):
    flat = tuple(v for pair in reversed(pw) for v in pair)
    return torch.nn.functional.pad(x, flat)

kernel_name = 'parallel_sum__softmax_full_mha__glu_gated_geglu__prompt'

def setup_inputs():

    _w = (lambda a: (lambda t: t.to(torch.bfloat16) if t.is_floating_point() else t)(torch.as_tensor(a)))
    def _A_setup():
        B, S, D = 1, 2048, 512
        return {
            "x": _w((lambda *s: torch.randn(*s, dtype=torch.bfloat16) * 0.05)(B, S, D)),
            "wqkv": _w((lambda *s: torch.randn(*s, dtype=torch.bfloat16) * 0.05)(D, 3 * D)),
            "wo": _w((lambda *s: torch.randn(*s, dtype=torch.bfloat16) * 0.05)(D, D)),
        }
    _A_in = _A_setup()

    def _B_setup():
        B, S, Dm, Df = 1, 2048, 512, 1792
        return {
            "x": _w((lambda *s: torch.randn(*s, dtype=torch.bfloat16) * 0.05)(B, S, Dm)),
            "w_gate": _w((lambda *s: torch.randn(*s, dtype=torch.bfloat16) * 0.4)(Dm, Df) * (Dm ** -0.5)),
            "w_up": _w((lambda *s: torch.randn(*s, dtype=torch.bfloat16) * 0.4)(Dm, Df) * (Dm ** -0.5)),
            "w_down": _w((lambda *s: torch.randn(*s, dtype=torch.bfloat16) * 0.4)(Df, Dm) * (Df ** -0.5)),
        }
    _B_in = _B_setup()

    return {
        "A_x": _A_in["x"],
        "A_wqkv": _A_in["wqkv"],
        "A_wo": _A_in["wo"],
        "B_x": _B_in["x"],
        "B_w_gate": _B_in["w_gate"],
        "B_w_up": _B_in["w_up"],
        "B_w_down": _B_in["w_down"],
    }

def reference(A_x, A_wqkv, A_wo, B_x, B_w_gate, B_w_up, B_w_down):
    def _A_ref(x, wqkv, wo):
        B, S, D = x.shape
        H = 32; Dh = D // H
        qkv = (lambda x, *d: x.permute(*d))((x @ wqkv).reshape(B, S, 3, H, Dh), 2, 0, 3, 1, 4)
        q, k, v = qkv[0], qkv[1], qkv[2]
        scale = Dh ** -0.5
        scores = torch.einsum("bhqd,bhkd->bhqk", q, k) * scale
        p = _softmax(scores, axis=-1)
        o = torch.einsum("bhqk,bhkd->bhqd", p, v)
        return (lambda x, *d: x.permute(*d))(o, 0, 2, 1, 3).reshape(B, S, D) @ wo

    def _B_ref(x, w_gate, w_up, w_down):
        g = torch.einsum("bsd,df->bsf", x, w_gate)
        u = torch.einsum("bsd,df->bsf", x, w_up)
        h = _gelu(g) * u
        return torch.einsum("bsf,fd->bsd", h, w_down)

    x = A_x
    xn = _layernorm(x, axis=-1)
    return x + _A_ref(xn, A_wqkv, A_wo) + _B_ref(xn, B_w_gate, B_w_up, B_w_down)
```

```cpp
#include <hip/hip_runtime.h>
#include <hip/hip_bf16.h>
#include <stdint.h>

typedef float f32x16 __attribute__((ext_vector_type(16)));
typedef float f32x4 __attribute__((ext_vector_type(4)));
typedef float f32x2 __attribute__((ext_vector_type(2)));
typedef short s16x8 __attribute__((ext_vector_type(8)));
typedef short s16x4 __attribute__((ext_vector_type(4)));
typedef __bf16 bf16x2 __attribute__((ext_vector_type(2)));
typedef unsigned u32x4 __attribute__((ext_vector_type(4)));
typedef unsigned u32x2 __attribute__((ext_vector_type(2)));
typedef __amdgpu_buffer_rsrc_t srd_t;
#define LDSP __attribute__((address_space(3)))
#define DI __device__ __forceinline__
#define MFMA(a, b, c) __builtin_amdgcn_mfma_f32_32x32x16_bf16((a), (b), (c), 0, 0, 0)
#define SBAR() __builtin_amdgcn_sched_barrier(0)

constexpr int SEQ = 2048, DM = 512, NH = 32, DF = 1792;
constexpr unsigned WS_XN = 0;
constexpr unsigned WS_Q = WS_XN + (2u << 20);
constexpr unsigned WS_KV = WS_Q + (2u << 20);
constexpr unsigned WS_O = WS_KV + (4u << 20);
constexpr unsigned WS_H = WS_O + (2u << 20);
constexpr unsigned WS_KN = WS_H + 64u * 112u * 1024u;
constexpr float QSCALE = 0.25f * 1.4426950408889634f;
constexpr float BOUND_LIMIT = 48.0f;

DI srd_t make_srd(const void* p, unsigned bytes) { return __builtin_amdgcn_make_buffer_rsrc((void*)p, (short)0, (int)bytes, 0x00020000); }
DI unsigned pk_bf16(float lo, float hi) { f32x2 v = {lo, hi}; bf16x2 b = __builtin_convertvector(v, bf16x2); return __builtin_bit_cast(unsigned, b); }
DI float dot2(unsigned a, unsigned b, float c) { return __builtin_amdgcn_fdot2_f32_bf16(__builtin_bit_cast(bf16x2, a), __builtin_bit_cast(bf16x2, b), c, false); }
DI float half_sum(float v) { auto r = __builtin_amdgcn_permlane32_swap(__float_as_uint(v), __float_as_uint(v), false, false); return __uint_as_float(r[0]) + __uint_as_float(r[1]); }
DI float half_max(float v) { auto r = __builtin_amdgcn_permlane32_swap(__float_as_uint(v), __float_as_uint(v), false, false); return fmaxf(__uint_as_float(r[0]), __uint_as_float(r[1])); }
DI float bf_lo(unsigned u) { return __uint_as_float(u << 16); }
DI float bf_hi(unsigned u) { return __uint_as_float(u & 0xffff0000u); }
DI void dma16(unsigned lds, unsigned voff, srd_t srd, unsigned soff) {
  asm volatile("s_mov_b32 m0, %0\n\ts_nop 0\n\tbuffer_load_dwordx4 %1, %2, %3 offen lds" ::"s"(lds), "v"(voff), "s"(srd), "s"(soff) : "memory");
}
template <int N> DI void wait_bar() { asm volatile("s_waitcnt vmcnt(%0) lgkmcnt(0)\n\ts_barrier" ::"i"(N) : "memory"); }
DI s16x8 lds_frag(LDSP const char* p) { return *(LDSP const s16x8*)p; }
DI s16x8 lds_tr_frag(LDSP const char* p0, LDSP const char* p1) {
  s16x4 a = __builtin_amdgcn_ds_read_tr16_b64_v4i16((LDSP s16x4*)p0), b = __builtin_amdgcn_ds_read_tr16_b64_v4i16((LDSP s16x4*)p1);
  return __builtin_shufflevector(a, b, 0, 1, 2, 3, 4, 5, 6, 7);
}
DI int wave_id() { int w = __builtin_amdgcn_readfirstlane(threadIdx.x >> 6); asm volatile("s_nop 4" : "+s"(w)); return w; }

constexpr int K1_SLOT = 28672, K1_NSLOT = 5, K1_STAT = K1_SLOT * K1_NSLOT;
constexpr int K1_LDS = K1_STAT + 2048 + 1024 + 8 * 256;

template <int NV>
DI void k1_body(const char* x, const char* wqkv, char* ws, char* lds_g, int rb, int cb, int w) {
  LDSP char* lds = (LDSP char*)lds_g;
  const unsigned lds0 = (unsigned)(uintptr_t)lds;
  const int lane = threadIdx.x & 63, r = lane & 31, hh = lane >> 5, rt = w & 3, kh = w >> 2;
  const srd_t sx = make_srd(x, SEQ * DM * 2), sw = make_srd(wqkv, DM * 3 * DM * 2);
  const int xrow = rb * 128 + cb * 8 + (lane & 7), xch = w * 8 + (lane >> 3);
  const u32x4 xkeep = *(const u32x4*)(x + xrow * 1024 + xch * 16);
  const unsigned voX = (unsigned)((rb * 128 + 8 * w + (lane >> 3)) * 1024 + (((lane & 7) ^ ((4 * w + (lane >> 4)) & 7)) * 16));
  const int n0 = 64 * w + lane, n1 = 64 * (8 + w) + lane;
  const unsigned voW0 = (unsigned)((n0 / 12) * 3072 + cb * 192 + (n0 % 12) * 16), voW1 = (unsigned)((n1 / 12) * 3072 + cb * 192 + (n1 % 12) * 16);
  auto issue = [&](int st) {
    const unsigned sl = lds0 + (unsigned)((st % K1_NSLOT) * K1_SLOT) + (unsigned)w * 1024u;
    dma16(sl, voX, sx, st * 128);
    dma16(sl + 8192, voX, sx, st * 128 + 65536);
    dma16(sl + 16384, voW0, sw, st * 64 * 3072);
    if (w < 4) dma16(sl + 16384 + 8192, voW1, sw, st * 64 * 3072);
  };
#pragma unroll
  for (int st = 0; st < 5; ++st) issue(st);
  const int swz = (r >> 1) & 7;
  LDSP const char* pX0 = lds + (32 * rt + r) * 128 + (((4 * kh + hh) ^ swz) * 16);
  LDSP const char* pX1 = lds + (32 * rt + r) * 128 + (((4 * kh + 2 + hh) ^ swz) * 16);
  LDSP const char* pW = lds + 16384 + (32 * kh + 8 * hh + ((lane & 15) >> 2)) * 192 + ((lane >> 4) & 1) * 32 + (lane & 3) * 8;
  f32x16 acc[3] = {};
  float s1 = 0.f, s2 = 0.f, cs = 0.f;
  const unsigned ONES = 0x3f803f80u;
#pragma unroll
  for (int st = 0; st < 8; ++st) {
    constexpr int young[8] = {4, 3, 3, 3, 3, 2, 1, 0};
    if (w < 4) {
      switch (young[st]) { case 4: wait_bar<16>(); break; case 3: wait_bar<12>(); break; case 2: wait_bar<8>(); break; case 1: wait_bar<4>(); break; default: wait_bar<0>(); }
    } else {
      switch (young[st]) { case 4: wait_bar<12>(); break; case 3: wait_bar<9>(); break; case 2: wait_bar<6>(); break; case 1: wait_bar<3>(); break; default: wait_bar<0>(); }
    }
    if (st >= 1 && st <= 3) issue(st + 4);
    const int so = (st % K1_NSLOT) * K1_SLOT;
#pragma unroll
    for (int e = 0; e < 2; ++e) {
      const s16x8 xf = lds_frag((e ? pX1 : pX0) + so);
      s16x8 wf[3];
#pragma unroll
      for (int j = 0; j < 3; ++j) wf[j] = lds_tr_frag(pW + so + e * 16 * 192 + 64 * j, pW + so + e * 16 * 192 + 4 * 192 + 64 * j);
#pragma unroll
      for (int j = 0; j < 3; ++j) acc[j] = (j >= 3 - NV) ? MFMA(xf, wf[j], acc[j]) : MFMA(wf[j], xf, acc[j]);
      const u32x4 xu = __builtin_bit_cast(u32x4, xf);
#pragma unroll
      for (int i = 0; i < 4; ++i) { s1 = dot2(xu[i], ONES, s1); s2 = dot2(xu[i], xu[i], s2); }
#pragma unroll
      for (int j = 0; j < 3; ++j)
        if (rt == j) {
          const u32x4 wu = __builtin_bit_cast(u32x4, wf[j]);
#pragma unroll
          for (int i = 0; i < 4; ++i) cs = dot2(wu[i], ONES, cs);
        }
    }
  }
  LDSP float* stat = (LDSP float*)(lds + K1_STAT);
  LDSP float* csum = (LDSP float*)(lds + K1_STAT + 2048);
  LDSP float* wscr = (LDSP float*)(lds + K1_STAT + 3072) + w * 64;
  s1 = half_sum(s1); s2 = half_sum(s2); cs = half_sum(cs);
  if (hh == 0) {
    *(LDSP f32x2*)(stat + (kh * 128 + 32 * rt + r) * 2) = f32x2{s1, s2};
    if (rt < 3) csum[kh * 128 + 32 * rt + r] = cs;
  }
  {
    LDSP char* ex = lds + 3 * K1_SLOT + w * 6144 + lane * 16;
    const f32x16& snd = kh ? acc[0] : acc[1];
#pragma unroll
    for (int g = 0; g < 4; ++g) *(LDSP f32x4*)(ex + g * 1024) = kh ? f32x4{acc[0][4 * g], acc[0][4 * g + 1], acc[0][4 * g + 2], acc[0][4 * g + 3]}
                                                                    : f32x4{acc[1][4 * g], acc[1][4 * g + 1], acc[1][4 * g + 2], acc[1][4 * g + 3]};
    (void)snd;
#pragma unroll
    for (int g = 0; g < 2; ++g) *(LDSP f32x4*)(ex + (4 + g) * 1024) = kh ? f32x4{acc[2][4 * g], acc[2][4 * g + 1], acc[2][4 * g + 2], acc[2][4 * g + 3]}
                                                                          : f32x4{acc[2][8 + 4 * g], acc[2][8 + 4 * g + 1], acc[2][8 + 4 * g + 2], acc[2][8 + 4 * g + 3]};
  }
  wait_bar<0>();
  float val[24];
  {
    LDSP const char* ex = lds + 3 * K1_SLOT + (w ^ 4) * 6144 + lane * 16;
#pragma unroll
    for (int g = 0; g < 6; ++g) {
      const f32x4 v = *(LDSP const f32x4*)(ex + g * 1024);
#pragma unroll
      for (int i = 0; i < 4; ++i) {
        const float own = g < 4 ? (kh ? acc[1][4 * g + i] : acc[0][4 * g + i]) : (kh ? acc[2][8 + 4 * (g - 4) + i] : acc[2][4 * (g - 4) + i]);
        val[4 * g + i] = own + v[i];
      }
    }
  }
  float ra, rbm;
  {
    const f32x2 p0 = *(LDSP const f32x2*)(stat + (32 * rt + r) * 2), p1 = *(LDSP const f32x2*)(stat + (128 + 32 * rt + r) * 2);
    const float mean = (p0[0] + p1[0]) * (1.0f / DM), var = fmaxf((p0[1] + p1[1]) * (1.0f / DM) - mean * mean, 0.f);
    ra = __builtin_amdgcn_rsqf(var + 1e-5f); rbm = -mean * ra;
  }
  if (NV > 0) { if (hh == 0) { wscr[r] = ra; wscr[32 + r] = rbm; } }
  const int tt = rb * 4 + rt;
#pragma unroll
  for (int u = 0; u < 3; ++u) {
    const int j = u < 2 ? kh : 2, e = u < 2 ? u : kh, ct = 3 * cb + j;
    const bool isV = (u < 2) ? (NV == 3) : (NV >= 1);
    float o[8];
    if (!isV) {
      const float sc = ct < 16 ? QSCALE : 1.0f, a = ra * sc, b = rbm * sc;
#pragma unroll
      for (int g = 0; g < 2; ++g) {
        const f32x4 c0 = *(LDSP const f32x4*)(csum + 32 * j + 16 * e + 8 * g + 4 * hh), c1 = *(LDSP const f32x4*)(csum + 128 + 32 * j + 16 * e + 8 * g + 4 * hh);
#pragma unroll
        for (int i = 0; i < 4; ++i) o[4 * g + i] = a * val[8 * u + 4 * g + i] + b * (c0[i] + c1[i]);
      }
      const u32x4 pk = {pk_bf16(o[0], o[1]), pk_bf16(o[2], o[3]), pk_bf16(o[4], o[5]), pk_bf16(o[6], o[7])};
      if (ct < 16) {
        *(u32x4*)(ws + WS_Q + ((2 * ct + e) * 64 + tt) * 1024 + lane * 16) = pk;
      } else {
        const int head = 2 * (ct - 16) + e;
        *(u32x4*)(ws + WS_KV + (head * 64 + tt) * 2048 + lane * 16) = pk;
        float n2 = 0.f;
#pragma unroll
        for (int i = 0; i < 4; ++i) n2 = dot2(pk[i], pk[i], n2);
        n2 = half_sum(n2);
#pragma unroll
        for (int m = 1; m < 32; m <<= 1) n2 = fmaxf(n2, __shfl_xor(n2, m, 64));
        if (lane == 0) ((float*)(ws + WS_KN))[head * 64 + tt] = n2;
      }
    } else {
      const float c = csum[32 * j + r] + csum[128 + 32 * j + r];
#pragma unroll
      for (int g = 0; g < 2; ++g) {
        const f32x4 a4 = *(LDSP const f32x4*)(wscr + 16 * e + 8 * g + 4 * hh), b4 = *(LDSP const f32x4*)(wscr + 32 + 16 * e + 8 * g + 4 * hh);
#pragma unroll
        for (int i = 0; i < 4; ++i) o[4 * g + i] = a4[i] * val[8 * u + 4 * g + i] + b4[i] * c;
      }
      const u32x4 pk = {pk_bf16(o[0], o[1]), pk_bf16(o[2], o[3]), pk_bf16(o[4], o[5]), pk_bf16(o[6], o[7])};
      const int head = 2 * (ct - 32) + (r >> 4);
      *(u32x4*)(ws + WS_KV + (head * 64 + tt) * 2048 + 1024 + e * 512 + hh * 256 + (r & 15) * 16) = pk;
    }
  }
  {
    const int lr = cb * 8 + (lane & 7);
    const f32x2 p0 = *(LDSP const f32x2*)(stat + lr * 2), p1 = *(LDSP const f32x2*)(stat + (128 + lr) * 2);
    const float mean = (p0[0] + p1[0]) * (1.0f / DM), var = fmaxf((p0[1] + p1[1]) * (1.0f / DM) - mean * mean, 0.f);
    const float a = __builtin_amdgcn_rsqf(var + 1e-5f), b = -mean * a;
    u32x4 pk;
#pragma unroll
    for (int i = 0; i < 4; ++i) pk[i] = pk_bf16(a * bf_lo(xkeep[i]) + b, a * bf_hi(xkeep[i]) + b);
    *(u32x4*)(ws + WS_XN + (((xrow >> 5) * 32 + (xch >> 1)) * 64 + (xch & 1) * 32 + (xrow & 31)) * 16) = pk;
  }
}

__global__ __launch_bounds__(512, 2) void k1_qkv(const char* __restrict__ x, const char* __restrict__ wqkv, char* __restrict__ ws) {
  extern __shared__ __attribute__((aligned(16))) char lds[];
  const int b = blockIdx.x, xcd = b & 7, i = b >> 3;
  const int rb = 4 * (xcd >> 1) + (i & 3), cb = 8 * (xcd & 1) + (i >> 2);
  const int w = wave_id();
  if (cb < 10) k1_body<0>(x, wqkv, ws, lds, rb, cb, w);
  else if (cb == 10) k1_body<1>(x, wqkv, ws, lds, rb, cb, w);
  else k1_body<3>(x, wqkv, ws, lds, rb, cb, w);
}

constexpr int AT_TILE = 2080, AT_SLOT = 8 * AT_TILE, AT_EX = 3 * AT_SLOT;
constexpr int K2_LDS = 73728;

#define EXP1(r) "v_exp_f32 v" #r ", v" #r "\n\t"
#define CVT1(d, a, b) "v_cvt_pk_bf16_f32 v" #d ", v" #a ", v" #b "\n\t"
#define MF(d, a, b, c) "v_mfma_f32_32x32x16_bf16 " d ", " a ", " b ", " c "\n\t"
#define R_OA "v[16:31]"
#define R_OB "v[32:47]"
#define R_SA "v[48:63]"
#define R_SB "v[64:79]"
#define R_C "v[80:95]"
#define R_QA "v[96:99]"
#define R_QB "v[100:103]"
#define R_K "v[104:107]"
#define R_V0 "v[108:111]"
#define R_V1 "v[112:115]"
#define R_P0 "v[116:119]"
#define R_P1 "v[120:123]"
#define UNIT_A(PRE, KOFF, VOFF)                                                                                     \
  MF(R_SB, R_K, R_QB, R_C) EXP1(48) EXP1(49) MF(R_OB, R_V1, R_P1, R_OB) PRE                                          \
  "ds_read_b128 " R_K ", %[aK] offset:" KOFF "\n\t"                                                                  \
  "ds_read_b128 " R_V1 ", %[aV1] offset:" VOFF "\n\t"                                                                \
  EXP1(50) EXP1(51) EXP1(52) EXP1(53) EXP1(54) EXP1(55) EXP1(56)                                                     \
  CVT1(116, 48, 49) CVT1(117, 50, 51) CVT1(118, 52, 53) CVT1(119, 54, 55) EXP1(57) EXP1(58)                          \
  "s_waitcnt lgkmcnt(2)\n\t" MF(R_OA, R_V0, R_P0, R_OA) EXP1(59) EXP1(60) EXP1(61) EXP1(62) EXP1(63)                 \
  CVT1(120, 56, 57) CVT1(121, 58, 59) CVT1(122, 60, 61) CVT1(123, 62, 63)
#define UNIT_B(PRE, VOFF)                                                                                            \
  "s_waitcnt lgkmcnt(0)\n\t" MF(R_SA, R_K, R_QA, R_C) EXP1(64) EXP1(65) MF(R_OA, R_V1, R_P1, R_OA)                    \
  EXP1(66) EXP1(67) EXP1(68) EXP1(69) EXP1(70) EXP1(71) EXP1(72)                                                     \
  CVT1(116, 64, 65) CVT1(117, 66, 67) CVT1(118, 68, 69) CVT1(119, 70, 71) EXP1(73) EXP1(74)                          \
  MF(R_OB, R_V0, R_P0, R_OB) PRE "ds_read_b128 " R_V0 ", %[aV0] offset:" VOFF "\n\t"                                 \
  EXP1(75) EXP1(76) EXP1(77) EXP1(78) EXP1(79)                                                                       \
  CVT1(120, 72, 73) CVT1(121, 74, 75) CVT1(122, 76, 77) CVT1(123, 78, 79)

DI void attn_body(char* ws, char* lds_g, int b, int w) {
  LDSP char* lds = (LDSP char*)lds_g;
  const unsigned lds0 = (unsigned)(uintptr_t)lds;
  const int lane = threadIdx.x & 63, r = lane & 31, hh = lane >> 5, qp = w & 3, kh = w >> 2;
  const int head = (b & 7) * 4 + ((b >> 3) & 3), qb = b >> 5;
  const srd_t skv = make_srd(ws + WS_KV + head * 131072, 131072);
  const unsigned voff = lane * 16, sbase = (unsigned)((32 * kh + qp) * 2048), mbase = lds0 + (unsigned)(w * AT_TILE);
  auto issue = [&](int c, int slot) {
    asm volatile("s_mov_b32 m0, %0\n\ts_nop 0\n\tbuffer_load_dwordx4 %1, %2, %3 offen lds\n\tbuffer_load_dwordx4 %1, %2, %3 offen offset:1024 lds"
                 ::"s"(mbase + (unsigned)(slot * AT_SLOT)), "v"(voff), "s"(skv), "s"(sbase + (unsigned)(c * 8192)) : "memory");
  };
  const int qt = qb * 8 + qp * 2;
  const u32x4 qa = *(const u32x4*)(ws + WS_Q + (head * 64 + qt) * 1024 + lane * 16);
  const u32x4 qbf = *(const u32x4*)(ws + WS_Q + (head * 64 + qt + 1) * 1024 + lane * 16);
  float kn = ((const float*)(ws + WS_KN))[head * 64 + lane];
  issue(0, 0); issue(1, 1);
  if (threadIdx.x < 48) {
    const unsigned v = (threadIdx.x & 1) ? 0u : 0x3f803f80u;
    *(LDSP u32x4*)(lds + (threadIdx.x >> 1) * AT_TILE + 2048 + (threadIdx.x & 1) * 16) = u32x4{v, v, v, v};
  }
#pragma unroll
  for (int m = 1; m < 64; m <<= 1) kn = fmaxf(kn, __shfl_xor(kn, m, 64));
  float na = 0.f, nb = 0.f;
#pragma unroll
  for (int i = 0; i < 4; ++i) { na = dot2(qa[i], qa[i], na); nb = dot2(qbf[i], qbf[i], nb); }
  const float bound = __builtin_sqrtf(fmaxf(half_sum(na), half_sum(nb)) * kn) * 1.001f;
  const bool safe = __all(bound <= BOUND_LIMIT);
  unsigned aK = lds0 + (unsigned)(kh * 4 * AT_TILE + lane * 16);
  const unsigned cst = (unsigned)(2048 + ((r == 16 || r == 20) ? 0 : 16));
  unsigned aV0 = lds0 + (unsigned)(kh * 4 * AT_TILE) + (r < 16 ? (unsigned)(1024 + hh * 256 + r * 16) : cst);
  unsigned aV1 = lds0 + (unsigned)(kh * 4 * AT_TILE) + (r < 16 ? (unsigned)(1536 + hh * 256 + r * 16) : cst);
  f32x16 oa = {}, ob = {};
  float ma = bound, mb = bound;
  wait_bar<2>();
  if (safe) {
    f32x16 cneg;
#pragma unroll
    for (int i = 0; i < 16; ++i) cneg[i] = -bound;
    unsigned cnt = 0, slot = 0, delta, m0n, soff = sbase + 2 * 8192;
    asm volatile(
        "ds_read_b128 " R_K ", %[aK]\n\t"
        "ds_read_b128 " R_V0 ", %[aV0]\n\t"
        "v_mov_b32 v112, 0\n\tv_mov_b32 v113, 0\n\tv_mov_b32 v114, 0\n\tv_mov_b32 v115, 0\n\t"
        "v_mov_b32 v120, 0\n\tv_mov_b32 v121, 0\n\tv_mov_b32 v122, 0\n\tv_mov_b32 v123, 0\n\t"
        "s_waitcnt lgkmcnt(0)\n\t" MF(R_SA, R_K, R_QA, R_C)
        "s_nop 15\n\t"
        "1:\n\t"
        "s_cmp_eq_u32 %[slot], 2\n\t"
        "s_cselect_b32 %[delta], %[cm2], %[cp1]\n\t"
        "s_cmp_eq_u32 %[slot], 0\n\t"
        "s_cselect_b32 %[m0n], %[cp2], %[cm1]\n\t"
        "s_mul_i32 %[slot], %[slot], %[cp1]\n\t"
        "s_add_u32 %[m0n], %[m0n], %[slot]\n\t"
        "s_add_u32 %[m0n], %[m0n], %[mbase]\n\t"
        UNIT_A("", "2080", "0") UNIT_B("", "2080")
        UNIT_A("", "4160", "2080") UNIT_B("", "4160")
        UNIT_A("", "6240", "4160") UNIT_B("", "6240")
        UNIT_A("s_waitcnt vmcnt(0)\n\ts_barrier\n\t"
               "s_cmp_gt_u32 %[cnt], 5\n\ts_cbranch_scc1 2f\n\t"
               "s_mov_b32 m0, %[m0n]\n\ts_nop 0\n\t"
               "buffer_load_dwordx4 %[voff], %[skv], %[soff] offen lds\n\t"
               "buffer_load_dwordx4 %[voff], %[skv], %[soff] offen offset:1024 lds\n\t"
               "2:\n\t"
               "v_add_u32 %[aK], %[delta], %[aK]\n\t", "0", "6240")
        UNIT_B("v_add_u32 %[aV0], %[delta], %[aV0]\n\t", "0")
        "v_add_u32 %[aV1], %[delta], %[aV1]\n\t"
        "s_add_u32 %[soff], %[soff], 8192\n\t"
        "s_add_u32 %[cnt], %[cnt], 1\n\t"
        "s_mul_hi_u32 %[slot], %[cnt], %[magic3]\n\t"
        "s_mul_i32 %[slot], %[slot], 3\n\t"
        "s_sub_u32 %[slot], %[cnt], %[slot]\n\t"
        "s_cmp_lt_u32 %[cnt], 8\n\t"
        "s_cbranch_scc1 1b\n\t"
        MF(R_OB, R_V1, R_P1, R_OB)
        "s_waitcnt lgkmcnt(0)\n\ts_nop 15\n\t"
        : "+{v[16:31]}"(oa), "+{v[32:47]}"(ob), [aK] "+v"(aK), [aV0] "+v"(aV0), [aV1] "+v"(aV1), [cnt] "+s"(cnt), [slot] "+s"(slot),
          [delta] "=&s"(delta), [m0n] "=&s"(m0n), [soff] "+s"(soff)
        : "{v[80:95]}"(cneg), "{v[96:99]}"(qa), "{v[100:103]}"(qbf), [voff] "v"(voff), [skv] "s"(skv), [mbase] "s"(mbase),
          [cp1] "s"(AT_SLOT), [cp2] "s"(2 * AT_SLOT), [cm1] "s"(-AT_SLOT), [cm2] "s"(-2 * AT_SLOT), [magic3] "s"(0x55555556u)
        : "memory", "scc", "v48", "v49", "v50", "v51", "v52", "v53", "v54", "v55", "v56", "v57", "v58", "v59", "v60", "v61", "v62", "v63", "v64",
          "v65", "v66", "v67", "v68", "v69", "v70", "v71", "v72", "v73", "v74", "v75", "v76", "v77", "v78", "v79", "v104", "v105", "v106",
          "v107", "v108", "v109", "v110", "v111", "v112", "v113", "v114", "v115", "v116", "v117", "v118", "v119", "v120", "v121", "v122", "v123");
  } else {
    ma = -INFINITY; mb = -INFINITY;
    const s16x8 qf[2] = {__builtin_bit_cast(s16x8, qa), __builtin_bit_cast(s16x8, qbf)};
    int slot = 0;
    for (int c = 0; c < 8; ++c) {
      for (int j = 0; j < 4; ++j) {
        const unsigned so = (unsigned)(slot * AT_SLOT + j * AT_TILE);
        const s16x8 kf = lds_frag((LDSP const char*)(uintptr_t)(aK + so));
        const s16x8 v0 = lds_frag((LDSP const char*)(uintptr_t)(aV0 + so)), v1 = lds_frag((LDSP const char*)(uintptr_t)(aV1 + so));
#pragma unroll
        for (int qi = 0; qi < 2; ++qi) {
          f32x16& o = qi ? ob : oa;
          float& m = qi ? mb : ma;
          f32x16 s = MFMA(kf, qf[qi], f32x16{});
          float tm = s[0];
#pragma unroll
          for (int i = 1; i < 16; ++i) tm = fmaxf(tm, s[i]);
          const float mn = fmaxf(m, half_max(tm));
          const float f = __builtin_amdgcn_exp2f(m - mn);
          m = mn;
#pragma unroll
          for (int i = 0; i < 16; ++i) { o[i] *= f; s[i] = __builtin_amdgcn_exp2f(s[i] - mn); }
          const u32x4 p0 = {pk_bf16(s[0], s[1]), pk_bf16(s[2], s[3]), pk_bf16(s[4], s[5]), pk_bf16(s[6], s[7])};
          const u32x4 p1 = {pk_bf16(s[8], s[9]), pk_bf16(s[10], s[11]), pk_bf16(s[12], s[13]), pk_bf16(s[14], s[15])};
          o = MFMA(v0, __builtin_bit_cast(s16x8, p0), o);
          o = MFMA(v1, __builtin_bit_cast(s16x8, p1), o);
        }
      }
      wait_bar<0>();
      if (c < 6) issue(c + 2, slot == 0 ? 2 : slot - 1);
      slot = slot == 2 ? 0 : slot + 1;
    }
  }
  {
    LDSP char* ex = lds + AT_EX + w * 2560;
    const f32x16& snd = kh ? oa : ob;
    *(LDSP f32x4*)(ex + lane * 16) = f32x4{snd[0], snd[1], snd[2], snd[3]};
    *(LDSP f32x4*)(ex + 1024 + lane * 16) = f32x4{snd[4], snd[5], snd[6], snd[7]};
    *(LDSP f32x2*)(ex + 2048 + lane * 8) = f32x2{snd[8], kh ? ma : mb};
  }
  wait_bar<0>();
  {
    LDSP const char* ex = lds + AT_EX + (w ^ 4) * 2560;
    const f32x16& own = kh ? ob : oa;
    const float mo = kh ? mb : ma;
    const f32x4 t0 = *(LDSP const f32x4*)(ex + lane * 16), t1 = *(LDSP const f32x4*)(ex + 1024 + lane * 16);
    const f32x2 t2 = *(LDSP const f32x2*)(ex + 2048 + lane * 8);
    const float mm = fmaxf(mo, t2[1]);
    const float f0 = __builtin_amdgcn_exp2f(mo - mm), f1 = __builtin_amdgcn_exp2f(t2[1] - mm);
    const float inv = 1.0f / (own[8] * f0 + t2[0] * f1);
    const float g0 = f0 * inv, g1 = f1 * inv;
    const u32x4 pk = {pk_bf16(own[0] * g0 + t0[0] * g1, own[1] * g0 + t0[1] * g1), pk_bf16(own[2] * g0 + t0[2] * g1, own[3] * g0 + t0[3] * g1),
                      pk_bf16(own[4] * g0 + t1[0] * g1, own[5] * g0 + t1[1] * g1), pk_bf16(own[6] * g0 + t1[2] * g1, own[7] * g0 + t1[3] * g1)};
    *(u32x4*)(ws + WS_O + ((qt + kh) * 32 + head) * 1024 + lane * 16) = pk;
  }
}

DI float geglu(float g, float u) {
  const float c1 = -2.0f * 0.7978845608028654f * 1.4426950408889634f, c2 = c1 * 0.044715f;
  const float e = __builtin_amdgcn_exp2f(g * (c1 + c2 * g * g));
  return g * u * __builtin_amdgcn_rcpf(1.0f + e);
}

DI void ffn_up_body(const char* wg, const char* wu, char* ws, char* lds_g, int j, int w) {
  LDSP char* lds = (LDSP char*)lds_g;
  const unsigned lds0 = (unsigned)(uintptr_t)lds;
  const int lane = threadIdx.x & 63, hh = lane >> 5, tp = w >> 2, ft = w & 3;
  const int xcd = j & 7, i = j >> 3, tb = 4 * (xcd >> 1) + (i & 3), fb = 7 * (xcd & 1) + (i >> 2);
  const srd_t sx = make_srd(ws + WS_XN, 2u << 20), sg = make_srd(wg, DM * DF * 2), su = make_srd(wu, DM * DF * 2);
  const unsigned voX = lane * 16, soX = (unsigned)(((tb * 4 + (w >> 1)) * 32 + (w & 1)) * 1024);
  const unsigned voW = (unsigned)((4 * w + (lane >> 4)) * (DF * 2) + fb * 256 + (((lane & 15) ^ (((lane >> 4) & 3) << 2)) * 16));
  auto issue = [&](int st) {
    const unsigned sl = lds0 + (unsigned)((st % 3) * 8192) + (unsigned)w * 1024u;
    dma16(sl, voX, sx, soX + st * 2048);
    dma16(sl + 24576, voW, sg, st * 32 * DF * 2);
    dma16(sl + 49152, voW, su, st * 32 * DF * 2);
  };
  issue(0); issue(1);
  const int q = (lane & 15) >> 2;
  LDSP const char* pX = lds + tp * 4096 + lane * 16;
  LDSP const char* pG = lds + 24576 + (8 * hh + q) * 256 + ((ft ^ q) * 64) + ((lane >> 4) & 1) * 32 + (lane & 3) * 8;
  LDSP const char* pU = pG + 24576;
  f32x16 ga = {}, gb = {}, ua = {}, ub = {};
#pragma unroll
  for (int st = 0; st < 16; ++st) {
    if (st < 15) wait_bar<3>(); else wait_bar<0>();
    if (st + 2 < 16) issue(st + 2);
    const int so = (st % 3) * 8192;
#pragma unroll
    for (int s = 0; s < 2; ++s) {
      const s16x8 xa = lds_frag(pX + so + s * 1024), xb = lds_frag(pX + so + 2048 + s * 1024);
      const s16x8 fg = lds_tr_frag(pG + so + s * 4096, pG + so + s * 4096 + 1024), fu = lds_tr_frag(pU + so + s * 4096, pU + so + s * 4096 + 1024);
      ga = MFMA(fg, xa, ga); gb = MFMA(fg, xb, gb); ua = MFMA(fu, xa, ua); ub = MFMA(fu, xb, ub);
    }
  }
  const int fs = fb * 8 + ft * 2;
#pragma unroll
  for (int t = 0; t < 2; ++t) {
    const f32x16& g = t ? gb : ga;
    const f32x16& u = t ? ub : ua;
#pragma unroll
    for (int e = 0; e < 2; ++e) {
      u32x4 pk;
#pragma unroll
      for (int k = 0; k < 4; ++k) pk[k] = pk_bf16(geglu(g[8 * e + 2 * k], u[8 * e + 2 * k]), geglu(g[8 * e + 2 * k + 1], u[8 * e + 2 * k + 1]));
      *(u32x4*)(ws + WS_H + ((tb * 4 + tp * 2 + t) * 112 + fs + e) * 1024 + lane * 16) = pk;
    }
  }
}

__global__ __launch_bounds__(512, 4) void k2_mix(const char* __restrict__ wg, const char* __restrict__ wu, char* __restrict__ ws) {
  extern __shared__ __attribute__((aligned(16))) char lds[];
  const int w = wave_id();
  if (blockIdx.x < 256) {
    __builtin_amdgcn_s_setprio(2);
    attn_body(ws, lds, blockIdx.x, w);
  } else {
    ffn_up_body(wg, wu, ws, lds, blockIdx.x - 256, w);
  }
}

constexpr int K3_NSLOT = 8, K3_LDS = K3_NSLOT * 16384;

__global__ __launch_bounds__(512, 2) void k3_out(const char* __restrict__ x, const char* __restrict__ wd, const char* __restrict__ wo,
                                                 const char* __restrict__ ws, char* __restrict__ out) {
  extern __shared__ __attribute__((aligned(16))) char lds_g[];
  LDSP char* lds = (LDSP char*)lds_g;
  const unsigned lds0 = (unsigned)(uintptr_t)lds;
  const int w = wave_id(), lane = threadIdx.x & 63, r = lane & 31, hh = lane >> 5;
  const int b = blockIdx.x, xcd = b & 7, i = b >> 3, mb = 4 * xcd + (i & 3), nb = i >> 2;
  const int tl = w & 1, nt = (w >> 1) & 1, kh = w >> 2;
  const int ocol = 64 * nb + 32 * nt + 16 * kh + 4 * hh, orow = 64 * mb + 32 * tl + r;
  const u32x2 xr0 = *(const u32x2*)(x + orow * 1024 + ocol * 2), xr1 = *(const u32x2*)(x + orow * 1024 + ocol * 2 + 16);
  const srd_t sh = make_srd(ws + WS_H, 64u * 112u * 1024u), so_ = make_srd(ws + WS_O, 2u << 20), sd = make_srd(wd, DF * DM * 2), sw = make_srd(wo, DM * DM * 2);
  const unsigned voX = lane * 16, soH = (unsigned)(((2 * mb + (w >> 2)) * 112 + (w & 3)) * 1024), soO = (unsigned)(((2 * mb + (w >> 2)) * 32 + (w & 3)) * 1024);
  const unsigned voW = (unsigned)((8 * w + (lane >> 3)) * 1024 + nb * 128 + (((lane & 7) ^ (((lane >> 4) & 1) << 2)) * 16));
  auto issue = [&](int st) {
    const unsigned sl = lds0 + (unsigned)((st % K3_NSLOT) * 16384) + (unsigned)w * 1024u;
    if (st < 28) { dma16(sl, voX, sh, soH + st * 4096); dma16(sl + 8192, voW, sd, st * 65536); }
    else { dma16(sl, voX, so_, soO + (st - 28) * 4096); dma16(sl + 8192, voW, sw, (st - 28) * 65536); }
  };
#pragma unroll
  for (int st = 0; st < 7; ++st) issue(st);
  const int q = (lane & 15) >> 2;
  LDSP const char* pX = lds + tl * 4096 + kh * 2048 + lane * 16;
  LDSP const char* pW = lds + 8192 + (32 * kh + 4 * hh + q) * 128 + ((nt ^ (q >> 1)) * 64) + ((lane >> 4) & 1) * 32 + (lane & 3) * 8;
  f32x16 acc = {};
#pragma unroll
  for (int st = 0; st < 36; ++st) {
    if (st <= 29) wait_bar<12>();
    else if (st == 30) wait_bar<10>(); else if (st == 31) wait_bar<8>(); else if (st == 32) wait_bar<6>();
    else if (st == 33) wait_bar<4>(); else if (st == 34) wait_bar<2>(); else wait_bar<0>();
    if (st + 7 < 36) issue(st + 7);
    const int so = (st % K3_NSLOT) * 16384;
    LDSP const char* bx = pX + (so & ~65535);
    LDSP const char* bw = pW + (so & ~65535);
    const int sl = so & 65535;
#pragma unroll
    for (int e = 0; e < 2; ++e) {
      const s16x8 xf = lds_frag(bx + sl + e * 1024);
      const s16x8 wf = lds_tr_frag(bw + sl + e * 2048, bw + sl + e * 2048 + 1024);
      acc = MFMA(wf, xf, acc);
    }
  }
  {
    LDSP char* ex = lds + w * 2048 + lane * 16;
    *(LDSP f32x4*)(ex) = kh ? f32x4{acc[0], acc[1], acc[2], acc[3]} : f32x4{acc[8], acc[9], acc[10], acc[11]};
    *(LDSP f32x4*)(ex + 1024) = kh ? f32x4{acc[4], acc[5], acc[6], acc[7]} : f32x4{acc[12], acc[13], acc[14], acc[15]};
  }
  wait_bar<0>();
  {
    LDSP const char* ex = lds + (w ^ 4) * 2048 + lane * 16;
    const f32x4 t0 = *(LDSP const f32x4*)(ex), t1 = *(LDSP const f32x4*)(ex + 1024);
    float v[8];
#pragma unroll
    for (int k = 0; k < 4; ++k) { v[k] = (kh ? acc[8 + k] : acc[k]) + t0[k]; v[4 + k] = (kh ? acc[12 + k] : acc[4 + k]) + t1[k]; }
    const u32x2 o0 = {pk_bf16(v[0] + bf_lo(xr0[0]), v[1] + bf_hi(xr0[0])), pk_bf16(v[2] + bf_lo(xr0[1]), v[3] + bf_hi(xr0[1]))};
    const u32x2 o1 = {pk_bf16(v[4] + bf_lo(xr1[0]), v[5] + bf_hi(xr1[0])), pk_bf16(v[6] + bf_lo(xr1[1]), v[7] + bf_hi(xr1[1]))};
    *(u32x2*)(out + orow * 1024 + ocol * 2) = o0;
    *(u32x2*)(out + orow * 1024 + ocol * 2 + 16) = o1;
  }
}

extern "C" void kernel_launch(void* const* d_in, const int* in_sizes, int n_in, void* d_out, int out_size, void* d_ws, size_t ws_size,
                              hipStream_t stream) {
  const char* x = (const char*)d_in[0];
  const char* wqkv = (const char*)d_in[1];
  const char* wo = (const char*)d_in[2];
  const char* wg = (const char*)d_in[4];
  const char* wu = (const char*)d_in[5];
  const char* wd = (const char*)d_in[6];
  char* ws = (char*)d_ws;
  (void)hipFuncSetAttribute((const void*)k1_qkv, hipFuncAttributeMaxDynamicSharedMemorySize, K1_LDS);
  (void)hipFuncSetAttribute((const void*)k2_mix, hipFuncAttributeMaxDynamicSharedMemorySize, K2_LDS);
  (void)hipFuncSetAttribute((const void*)k3_out, hipFuncAttributeMaxDynamicSharedMemorySize, K3_LDS);
  k1_qkv<<<256, 512, K1_LDS, stream>>>(x, wqkv, ws);
  k2_mix<<<480, 512, K2_LDS, stream>>>(wg, wu, ws);
  k3_out<<<256, 512, K3_LDS, stream>>>(x, wd, wo, ws, (char*)d_out);
}
```

```cpp
#if defined(__HIP_DEVICE_COMPILE__)
#pragma clang attribute push(__attribute__((target("no-packed-fp32-ops"))), apply_to = function)
#endif
#include <hip/hip_runtime.h>
#include <stdint.h>

typedef float f32x16 __attribute__((ext_vector_type(16)));
typedef float f32x4 __attribute__((ext_vector_type(4)));
typedef float f32x2 __attribute__((ext_vector_type(2)));
typedef short s16x8 __attribute__((ext_vector_type(8)));
typedef short s16x4 __attribute__((ext_vector_type(4)));
typedef __bf16 bf16x2 __attribute__((ext_vector_type(2)));
typedef unsigned u32x4 __attribute__((ext_vector_type(4)));
typedef unsigned u32x2 __attribute__((ext_vector_type(2)));
typedef unsigned long long u64;
typedef __attribute__((address_space(1))) u64 gu64;
typedef __attribute__((address_space(1))) unsigned gu32;
typedef __amdgpu_buffer_rsrc_t srd_t;
#define LDSP __attribute__((address_space(3)))
#define DI __device__ __forceinline__
#define MFMA(a, b, c) __builtin_amdgcn_mfma_f32_32x32x16_bf16((a), (b), (c), 0, 0, 0)
#define SBAR() __builtin_amdgcn_sched_barrier(0)
#define TID ((int)__builtin_amdgcn_workitem_id_x())
#define BID ((int)__builtin_amdgcn_workgroup_id_x())
#define POLL_SLEEP 4
#define HINT_AT 0
#define HINT_ST 3

constexpr int SEQ = 2048, DM = 512, DF = 1792;
constexpr unsigned WS_XN = 0;
constexpr unsigned WS_Q = WS_XN + (2u << 20);
constexpr unsigned WS_KV = WS_Q + (2u << 20);
constexpr unsigned WS_O = WS_KV + (4u << 20);
constexpr unsigned WS_H = WS_O + (2u << 20);
constexpr unsigned WS_KN = WS_H + 64u * 112u * 1024u;
constexpr unsigned WS_GS = WS_KN + 16384;
constexpr unsigned WS_FQ = WS_GS + 16384;
constexpr unsigned WS_FL = WS_FQ + 2048;
constexpr unsigned WS_FH = WS_FL + 2048;
constexpr unsigned WS_FO = WS_FH + 2048;
constexpr unsigned WS_FS = WS_FO + 2048;
constexpr unsigned WS_EP = WS_FS + 2048;
constexpr unsigned WS_END = WS_EP + 256;
constexpr float QSCALE = 0.25f * 1.4426950408889634f;
constexpr float BOUND_LIMIT = 48.0f;
constexpr unsigned OOB = 0x40000000u;
constexpr int LDS_BYTES = 81920;

DI bool all_lanes(bool c) { return __builtin_amdgcn_ballot_w64(!c) == 0; }
DI srd_t make_srd(const void* p, unsigned bytes) { return __builtin_amdgcn_make_buffer_rsrc((void*)p, (short)0, (int)bytes, 0x00020000); }
DI unsigned pk_bf16(float lo, float hi) { f32x2 v = {lo, hi}; bf16x2 b = __builtin_convertvector(v, bf16x2); return __builtin_bit_cast(unsigned, b); }
DI float rbf(float v) { return __uint_as_float(pk_bf16(v, v) << 16); }
DI float bf_lo(unsigned u) { return __uint_as_float(u << 16); }
DI float bf_hi(unsigned u) { return __uint_as_float(u & 0xffff0000u); }
template <bool MAX> DI float comb(float a, float b) { return MAX ? fmaxf(a, b) : a + b; }
template <bool MAX, int CTRL> DI float red_dpp(float v) { return comb<MAX>(v, __int_as_float(__builtin_amdgcn_update_dpp(0, __float_as_int(v), CTRL, 0xf, 0xf, true))); }
template <bool MAX> DI float red_x16(float v) { auto r = __builtin_amdgcn_permlane16_swap(__float_as_uint(v), __float_as_uint(v), false, false); return comb<MAX>(__uint_as_float(r[0]), __uint_as_float(r[1])); }
template <bool MAX> DI float red_x32(float v) { auto r = __builtin_amdgcn_permlane32_swap(__float_as_uint(v), __float_as_uint(v), false, false); return comb<MAX>(__uint_as_float(r[0]), __uint_as_float(r[1])); }
template <bool MAX> DI float red32(float v) {
  v = red_dpp<MAX, 0xB1>(v); v = red_dpp<MAX, 0x4E>(v); v = red_dpp<MAX, 0x141>(v); v = red_dpp<MAX, 0x140>(v); return red_x16<MAX>(v);
}
DI void dma16(unsigned lds, unsigned voff, srd_t srd, unsigned soff) {
  asm volatile("s_mov_b32 m0, %0\n\ts_nop 0\n\tbuffer_load_dwordx4 %1, %2, %3 offen lds" ::"s"(lds), "v"(voff), "s"(srd), "s"(soff) : "memory");
}
DI void dma16c(unsigned lds, unsigned voff, srd_t srd, unsigned soff) {
  asm volatile("s_mov_b32 m0, %0\n\ts_nop 0\n\tbuffer_load_dwordx4 %1, %2, %3 offen sc1 lds" ::"s"(lds), "v"(voff), "s"(srd), "s"(soff) : "memory");
}
template <int N> DI void wait_bar() { asm volatile("s_waitcnt vmcnt(%0) lgkmcnt(0)\n\ts_barrier" ::"i"(N) : "memory"); }
DI void lds_bar() { asm volatile("s_waitcnt lgkmcnt(0)\n\ts_barrier" ::: "memory"); }
DI s16x8 lds_frag(unsigned a) { return *(LDSP const s16x8*)(uintptr_t)a; }
DI s16x8 lds_tr_frag(unsigned a0, unsigned a1) {
  s16x4 a = __builtin_amdgcn_ds_read_tr16_b64_v4i16((LDSP s16x4*)(uintptr_t)a0), b = __builtin_amdgcn_ds_read_tr16_b64_v4i16((LDSP s16x4*)(uintptr_t)a1);
  return __builtin_shufflevector(a, b, 0, 1, 2, 3, 4, 5, 6, 7);
}
template <class T> DI T lds_ld(unsigned a) { return *(LDSP const T*)(uintptr_t)a; }
template <class T> DI void lds_st(unsigned a, T v) { *(LDSP T*)(uintptr_t)a = v; }
DI void st16c(srd_t srd, unsigned voff, u32x4 v) { __builtin_amdgcn_raw_buffer_store_b128(v, srd, voff, 0, 16); }
DI u64 make_tag(unsigned e) { return ((u64)(e ^ 0x5bd1e995u) << 32) | e; }
DI unsigned get_epoch(const char* ws) {
  unsigned e;
  asm volatile("s_load_dword %0, %1, %2\n\ts_waitcnt lgkmcnt(0)" : "=s"(e) : "s"(ws), "s"(WS_EP) : "memory");
  return e + 1u;
}
DI void flag_store(char* ws, unsigned off, u64 tag) { __hip_atomic_store((gu64*)(ws + off), tag, __ATOMIC_RELAXED, __HIP_MEMORY_SCOPE_AGENT); }
DI void publish(char* ws, unsigned off, u64 tag) { wait_bar<0>(); if (TID == 0) flag_store(ws, off, tag); }
DI void poll(char* ws, unsigned off, u64 tag) {
#pragma unroll 1
  for (int it = 0; it < (1 << 20); ++it) {
    const u64 v = __hip_atomic_load((gu64*)(ws + off), __ATOMIC_RELAXED, __HIP_MEMORY_SCOPE_AGENT);
    if (all_lanes(v == tag)) break;
    __builtin_amdgcn_s_sleep(POLL_SLEEP);
  }
}

#define STAMP_DECL do {} while (0)
#define STAMP(i) do {} while (0)
#define STAMP_FLUSH(ws) do {} while (0)
#define STAMP_ARG
#define STAMP_PASS

constexpr unsigned Q_W = 49152, Q_STAT = Q_W + 5 * 6144, Q_CS = Q_STAT + 1024;

DI void qkv_loader(const char* x, const char* wqkv, char* ws, unsigned lds0, int b, int rb, int cb, int w, unsigned& epoch STAMP_ARG) {
  const int lane = TID & 63, L = w - 4;
  const srd_t sx = make_srd(x, SEQ * DM * 2), sw = make_srd(wqkv, DM * 3 * DM * 2);
  const unsigned voX0 = (unsigned)((rb * 128 + 32 * L + (lane >> 3)) * 1024 + (((lane & 7) ^ (lane >> 4)) * 16));
  const unsigned voX1 = (unsigned)((rb * 128 + 32 * L + 8 + (lane >> 3)) * 1024 + (((lane & 7) ^ (4 + (lane >> 4))) * 16)) - 1024u;
  const unsigned voW0 = (unsigned)((8 * L + lane / 12) * 3072 + cb * 192 + (lane % 12) * 16), voW1 = voW0 + 4 * 3072 - 768;
  auto group = [&](unsigned hslot, unsigned hs, unsigned xslot, unsigned st, int part) {
    asm volatile("s_mov_b32 m0, %0\n\ts_mov_b32 exec_hi, 0xffff\n\tbuffer_load_dwordx4 %1, %3, %4 offen lds\n\tbuffer_load_dwordx4 %2, %3, %4 offen offset:768 lds\n\t"
                 "s_mov_b32 exec_hi, -1"
                 ::"s"(lds0 + Q_W + hslot + L * 1536), "v"(voW0), "v"(voW1), "s"(sw), "s"(hs * 98304) : "memory");
    asm volatile("s_mov_b32 m0, %0\n\ts_nop 0\n\tbuffer_load_dwordx4 %1, %3, %4 offen lds\n\tbuffer_load_dwordx4 %2, %3, %4 offen offset:1024 lds"
                 ::"s"(lds0 + xslot + L * 4096 + part * 2048), "v"(voX0), "v"(voX1), "s"(sx), "s"(st * 128 + part * 16384) : "memory");
  };
  group(0, 0, 0, 0, 0); group(6144, 1, 0, 0, 1); group(2 * 6144, 2, 16384, 1, 0); group(3 * 6144, 3, 16384, 1, 1);
  epoch = get_epoch(ws);
  STAMP(1);
#pragma unroll
  for (int st = 0; st < 6; ++st) {
    wait_bar<8>();
    group(((2 * st + 4) % 5) * 6144, 2 * st + 4, ((st + 2) % 3) * 16384, st + 2, 0);
    wait_bar<12>();
    group(((2 * st + 5) % 5) * 6144, 2 * st + 5, ((st + 2) % 3) * 16384, st + 2, 1);
    if (HINT_AT == 0 && st == HINT_ST && TID == 448) flag_store(ws, WS_FS + b * 8, make_tag(epoch));
  }
  wait_bar<8>();
  const srd_t sws = make_srd(ws, WS_END);
  const unsigned go = WS_GS + rb * 1024 + lane * 16;
  u32x4 gs;
  asm volatile("buffer_load_dwordx4 %0, %1, %2, 0 offen sc1" : "=v"(gs) : "v"(go), "s"(sws) : "memory");
  wait_bar<9>();
  asm volatile("s_waitcnt vmcnt(0)\n\ts_barrier\n\ts_barrier" : "+v"(gs)::"memory");
  if (L == 0) {
#pragma unroll 1
    for (int it = 0; it < (1 << 16) && !all_lanes(gs[1] == ~epoch && gs[3] == ~epoch); ++it) gs = __builtin_amdgcn_raw_buffer_load_b128(sws, go, 0, 16);
    lds_st<f32x4>(lds0 + Q_STAT + lane * 16, f32x4{bf_hi(gs[0]), __builtin_amdgcn_rcpf(bf_lo(gs[0])), bf_hi(gs[2]), __builtin_amdgcn_rcpf(bf_lo(gs[2]))});
  }
  STAMP(3);
  lds_bar();
}

template <int NV>
DI void qkv_wave(char* ws, unsigned lds0, int rb, int cb, int rt, unsigned& epoch STAMP_ARG) {
  const int lane = TID & 63, r = lane & 31, hh = lane >> 5;
  epoch = get_epoch(ws);
  const int swz = (r >> 1) & 7, sw = rt & 1;
  unsigned aX[4];
#pragma unroll
  for (int s = 0; s < 4; ++s) aX[s] = lds0 + (unsigned)((32 * rt + r) * 128 + (((2 * (s ^ sw) + hh) ^ swz) * 16));
  const unsigned aWb = lds0 + Q_W + (unsigned)((8 * hh + ((lane & 15) >> 2)) * 192 + ((lane >> 4) & 1) * 32 + (lane & 3) * 8);
  const unsigned aW[2] = {aWb + sw * 3072, aWb + (sw ^ 1) * 3072};
  f32x16 acc[3] = {};
  f32x4 csum[3] = {};
  s16x4 ones = {0x3f80, 0x3f80, 0x3f80, 0x3f80};
  asm volatile("" : "+v"(ones));
  s16x8 xf[3];
  s16x4 wl[3][3], wh[3][3];
#define TRD(A) __builtin_amdgcn_ds_read_tr16_b64_v4i16((LDSP s16x4*)(uintptr_t)(A))
#define QRX(F, S) xf[F] = lds_frag(aX[S] + xs)
#define QRL(F, J) wl[F][J] = TRD(aW[(F) != 0] + hr + 64 * (J))
#define QRH(F, J) wh[F][J] = TRD(aW[(F) != 0] + hr + 768 + 64 * (J))
#define QM(F, J) do { const s16x8 w_ = __builtin_shufflevector(wl[F][J], wh[F][J], 0, 1, 2, 3, 4, 5, 6, 7); \
    acc[J] = (J) >= 3 - NV ? MFMA(xf[F], w_, acc[J]) : MFMA(w_, xf[F], acc[J]); } while (0)
#define QC(J) do { csum[J] = __builtin_amdgcn_mfma_f32_4x4x4bf16_1k(ones, wl[0][J], csum[J], 0, 0, 0); } while (0)
#define QD(J) do { csum[J] = __builtin_amdgcn_mfma_f32_4x4x4bf16_1k(ones, wh[0][J], csum[J], 0, 0, 0); } while (0)
#define QHALF(H, S, N, P) do { const unsigned xs = (((H) >> 1) % 3) * 16384, hr = ((H) % 5) * 6144; lds_bar(); SBAR(); \
    if ((H) > 0) { QC(0); QC(1); QC(2); QD(0); QD(1); QD(2); SBAR(); QM(0, 0); SBAR(); } QRX(N, (S) + 1); QRL(N, 0); QRH(N, 0); QRL(0, 0); SBAR(); \
    if ((H) > 0) { QM(0, 1); SBAR(); } QRH(0, 0); QRL(N, 1); QRH(N, 1); QRL(0, 1); SBAR(); \
    if ((H) > 0) { QM(0, 2); SBAR(); } QRH(0, 1); QRX(0, S); QRL(0, 2); SBAR(); \
    if ((H) > 0) { QM(P, 0); SBAR(); } QRH(0, 2); QRL(N, 2); QRH(N, 2); SBAR(); \
    if ((H) > 0) { QM(P, 1); QM(P, 2); SBAR(); } } while (0)
  STAMP(2);
#pragma unroll
  for (int st = 0; st < 8; ++st) {
    QHALF(2 * st, 0, 1, 2);
    QHALF(2 * st + 1, 2, 2, 1);
    if (st == 0) STAMP(8);
    if (st == 3) STAMP(9);
    if (st == 5) STAMP(10);
  }
  QC(0); QC(1); QC(2); QD(0); QD(1); QD(2);
  QM(0, 0); QM(0, 1); QM(0, 2); QM(2, 0); QM(2, 1); QM(2, 2);
#undef QD
#undef QHALF
#undef QC
#undef QM
#undef QRL
#undef QRH
#undef QRX
#undef TRD
  if (rt < 2) {
#pragma unroll
    for (int j = 0; j < 3; ++j) { const float c = red_x32<false>(csum[j][0]); if (hh == 0) lds_st<float>(lds0 + Q_CS + rt * 384 + (32 * j + r) * 4, c); }
  }
  STAMP(3);
  lds_bar();
  const srd_t sws = make_srd(ws, WS_END);
  const int tt = rb * 4 + rt;
  const f32x2 ma = lds_ld<f32x2>(lds0 + Q_STAT + (32 * rt + r) * 8);
#pragma unroll
  for (int j = 0; j < 3; ++j) {
    const int ct = 3 * cb + j;
    if (j < 3 - NV) {
      const bool isq = ct < 16;
      const float a = ma[1] * (isq ? QSCALE : 1.0f), nm = -ma[0];
      const unsigned obase = (isq ? WS_Q + (unsigned)((2 * ct * 64 + tt) * 1024) : WS_KV + (unsigned)((2 * (ct - 16) * 64 + tt) * 2048)) + lane * 16;
      float n2[2];
#pragma unroll
      for (int e = 0; e < 2; ++e) {
        const unsigned ca = lds0 + Q_CS + (32 * j + 16 * e + 4 * hh) * 4;
        const f32x4 c0 = lds_ld<f32x4>(ca) + lds_ld<f32x4>(ca + 384), c1 = lds_ld<f32x4>(ca + 32) + lds_ld<f32x4>(ca + 32 + 384);
        float o[8], q2 = 0.f;
#pragma unroll
        for (int i = 0; i < 8; ++i) { o[i] = a * __builtin_fmaf(nm, i < 4 ? c0[i] : c1[i - 4], acc[j][8 * e + i]); q2 = __builtin_fmaf(o[i], o[i], q2); }
        st16c(sws, obase + e * (isq ? 65536u : 131072u), u32x4{pk_bf16(o[0], o[1]), pk_bf16(o[2], o[3]), pk_bf16(o[4], o[5]), pk_bf16(o[6], o[7])});
        n2[e] = q2;
      }
      auto sw2 = __builtin_amdgcn_permlane32_swap(__float_as_uint(n2[0]), __float_as_uint(n2[1]), false, false);
      const float kn = red32<true>(__uint_as_float(sw2[0]) + __uint_as_float(sw2[1]));
      if (r == 0) __hip_atomic_store((gu32*)(ws + WS_KN + ((2 * ct + hh) * 64 + tt) * 4), __float_as_uint(kn), __ATOMIC_RELAXED, __HIP_MEMORY_SCOPE_AGENT);
    } else {
      const float c = lds_ld<float>(lds0 + Q_CS + (32 * j + r) * 4) + lds_ld<float>(lds0 + Q_CS + 384 + (32 * j + r) * 4);
      const unsigned obase = WS_KV + (unsigned)(((2 * (ct - 32) + (r >> 4)) * 64 + tt) * 2048 + 1024 + hh * 256 + (r & 15) * 16);
#pragma unroll
      for (int e = 0; e < 2; ++e) {
        u32x4 pk;
#pragma unroll
        for (int g = 0; g < 2; ++g) {
          const unsigned sa = lds0 + Q_STAT + (32 * rt + 16 * e + 8 * g + 4 * hh) * 8;
          const f32x4 m0 = lds_ld<f32x4>(sa), m1 = lds_ld<f32x4>(sa + 16);
          const int i0 = 8 * e + 4 * g;
          pk[2 * g] = pk_bf16(m0[1] * __builtin_fmaf(-m0[0], c, acc[j][i0]), m0[3] * __builtin_fmaf(-m0[2], c, acc[j][i0 + 1]));
          pk[2 * g + 1] = pk_bf16(m1[1] * __builtin_fmaf(-m1[0], c, acc[j][i0 + 2]), m1[3] * __builtin_fmaf(-m1[2], c, acc[j][i0 + 3]));
        }
        st16c(sws, obase + e * 512, pk);
      }
    }
  }
}

constexpr int AT_TILE = 2080, AT_SLOT = 8 * AT_TILE, AT_EX = 3 * AT_SLOT;

#define EXP1(r) "v_exp_f32 v" #r ", v" #r "\n\t"
#define CVT1(d, a, b) "v_cvt_pk_bf16_f32 v" #d ", v" #a ", v" #b "\n\t"
#define MF(d, a, b, c) "v_mfma_f32_32x32x16_bf16 " d ", " a ", " b ", " c "\n\t"
#define R_OA "v[16:31]"
#define R_OB "v[32:47]"
#define R_SA "v[48:63]"
#define R_SB "v[64:79]"
#define R_C "v[80:95]"
#define R_QA "v[96:99]"
#define R_QB "v[100:103]"
#define R_K "v[104:107]"
#define R_V0 "v[108:111]"
#define R_V1 "v[112:115]"
#define R_P0 "v[116:119]"
#define R_P1 "v[120:123]"
#define PRHI "s_setprio 3\n\t"
#define PRLO "s_setprio 2\n\t"
#define UNIT_A(PRE, KOFF, VOFF)                                                                                      \
  PRHI MF(R_SB, R_K, R_QB, R_C) EXP1(48) PRE "ds_read_b128 " R_K ", %[aK] offset:" KOFF "\n\t"                       \
  EXP1(49) EXP1(50) EXP1(51) EXP1(52) MF(R_OB, R_V1, R_P1, R_OB) EXP1(53)                                            \
  "ds_read_b128 " R_V1 ", %[aV1] offset:" VOFF "\n\t" EXP1(54) EXP1(55) EXP1(56)                                     \
  CVT1(116, 48, 49) CVT1(117, 50, 51) CVT1(118, 52, 53) CVT1(119, 54, 55) EXP1(57)                                   \
  "s_waitcnt lgkmcnt(2)\n\t" EXP1(58) PRLO MF(R_OA, R_V0, R_P0, R_OA) EXP1(59) EXP1(60) EXP1(61) EXP1(62)            \
  "s_waitcnt lgkmcnt(0)\n\t" EXP1(63)                                                                                \
  CVT1(120, 56, 57) CVT1(121, 58, 59) CVT1(122, 60, 61) CVT1(123, 62, 63)
#define UNIT_B(PRE, VOFF)                                                                                            \
  PRHI MF(R_SA, R_K, R_QA, R_C) EXP1(64) EXP1(65) EXP1(66) EXP1(67) EXP1(68) MF(R_OA, R_V1, R_P1, R_OA)              \
  EXP1(69) EXP1(70) EXP1(71) EXP1(72)                                                                                \
  CVT1(116, 64, 65) CVT1(117, 66, 67) CVT1(118, 68, 69) CVT1(119, 70, 71) EXP1(73) EXP1(74)                          \
  PRLO MF(R_OB, R_V0, R_P0, R_OB) EXP1(75) PRE "ds_read_b128 " R_V0 ", %[aV0] offset:" VOFF "\n\t"                   \
  EXP1(76) EXP1(77) EXP1(78) EXP1(79)                                                                                \
  CVT1(120, 72, 73) CVT1(121, 74, 75) CVT1(122, 76, 77) CVT1(123, 78, 79)

DI void attn_phase(char* ws, unsigned lds0, int b, int w, u64 tag STAMP_ARG) {
  const int lane = TID & 63, r = lane & 31, hh = lane >> 5, qp = w & 3, kh = w >> 2;
  const int head = (b & 7) * 4 + ((b >> 3) & 3), qb = b >> 5;
  if (TID >= 64 && TID < 112) {
    const unsigned i = TID - 64, v = (i & 1) ? 0u : 0x3f803f80u;
    lds_st<u32x4>(lds0 + (i >> 1) * AT_TILE + 2048 + (i & 1) * 16, u32x4{v, v, v, v});
  }
  if (w == 0) {
    const int hp = head >> 1;
    const int f = lane < 16 ? ((16 + hp) / 3) * 16 + lane : lane < 32 ? ((32 + hp) / 3) * 16 + (lane - 16) : (hp / 3) * 16 + 2 * qb + (lane & 1);
    poll(ws, WS_FQ + f * 8, tag);
  }
  lds_bar();
  if (HINT_AT == 2 && TID == 0) flag_store(ws, WS_FS + b * 8, tag);
  STAMP(5);
  const srd_t skv = make_srd(ws + WS_KV + head * 131072, 131072), sws = make_srd(ws, WS_END);
  const unsigned voff = lane * 16, sbase = (unsigned)((32 * kh + qp) * 2048), mbase = lds0 + (unsigned)(w * AT_TILE);
  auto issue = [&](int c, int slot) {
    asm volatile("s_mov_b32 m0, %0\n\ts_nop 0\n\tbuffer_load_dwordx4 %1, %2, %3 offen sc1 lds\n\tbuffer_load_dwordx4 %1, %2, %3 offen offset:1024 sc1 lds"
                 ::"s"(mbase + (unsigned)(slot * AT_SLOT)), "v"(voff), "s"(skv), "s"(sbase + (unsigned)(c * 8192)) : "memory");
  };
  const int qt = qb * 8 + qp * 2;
  const u32x4 qa = __builtin_amdgcn_raw_buffer_load_b128(sws, voff, WS_Q + (head * 64 + qt) * 1024, 16);
  const u32x4 qbf = __builtin_amdgcn_raw_buffer_load_b128(sws, voff, WS_Q + (head * 64 + qt + 1) * 1024, 16);
  const float kn = __uint_as_float(__builtin_amdgcn_raw_buffer_load_b32(sws, lane * 4, WS_KN + (32 + head) * 256, 16));
  const u32x2 qn = __builtin_amdgcn_raw_buffer_load_b64(sws, 0, WS_KN + head * 256 + qt * 4, 16);
  issue(0, 0);
  const float bound = __builtin_amdgcn_sqrtf(fmaxf(__uint_as_float(qn[0]), __uint_as_float(qn[1])) * red_x32<true>(red32<true>(kn))) * 1.01f;
  const bool safe = all_lanes(bound <= BOUND_LIMIT);
  issue(1, 1);
  unsigned aK = lds0 + (unsigned)(kh * 4 * AT_TILE + lane * 16);
  const unsigned cst = (unsigned)(2048 + ((r == 16 || r == 20) ? 0 : 16));
  unsigned aV0 = lds0 + (unsigned)(kh * 4 * AT_TILE) + (r < 16 ? (unsigned)(1024 + hh * 256 + r * 16) : cst);
  unsigned aV1 = lds0 + (unsigned)(kh * 4 * AT_TILE) + (r < 16 ? (unsigned)(1536 + hh * 256 + r * 16) : cst);
  f32x16 oa = {}, ob = {};
  float ma = bound, mb = bound;
  wait_bar<2>();
  STAMP(6);
  if (safe) {
    f32x16 cneg;
#pragma unroll
    for (int i = 0; i < 16; ++i) cneg[i] = -bound;
    unsigned cnt = 0, delta = AT_SLOT, m0n = mbase + 2 * AT_SLOT, soff = sbase + 2 * 8192, ph = 0;
    asm volatile(
        "ds_read_b128 " R_K ", %[aK]\n\t"
        "ds_read_b128 " R_V0 ", %[aV0]\n\t"
        "v_mov_b32 v112, 0\n\tv_mov_b32 v113, 0\n\tv_mov_b32 v114, 0\n\tv_mov_b32 v115, 0\n\t"
        "v_mov_b32 v120, 0\n\tv_mov_b32 v121, 0\n\tv_mov_b32 v122, 0\n\tv_mov_b32 v123, 0\n\t"
        "s_waitcnt lgkmcnt(0)\n\t" MF(R_SA, R_K, R_QA, R_C)
        "s_nop 15\n\t"
        ".p2align 6\n\t"
        "1:\n\t"
        UNIT_A("", "2080", "0") UNIT_B("", "2080")
        UNIT_A("", "4160", "2080") UNIT_B("", "4160")
        UNIT_A("", "6240", "4160") UNIT_B("", "6240")
        UNIT_A("s_waitcnt vmcnt(0)\n\ts_barrier\n\tv_add_u32 %[aK], %[delta], %[aK]\n\t", "0", "6240")
        "s_mov_b32 m0, %[m0n]\n\t"
        UNIT_B("buffer_load_dwordx4 %[voff], %[skv], %[soff] offen sc1 lds\n\t"
               "v_add_u32 %[aV0], %[delta], %[aV0]\n\t"
               "buffer_load_dwordx4 %[voff], %[skv], %[soff] offen offset:1024 sc1 lds\n\t", "0")
        "v_add_u32 %[aV1], %[delta], %[aV1]\n\t"
        "s_add_u32 %[ph], %[ph], 1\n\t"
        "s_cmp_eq_u32 %[ph], 3\n\t"
        "s_cselect_b32 %[ph], 0, %[ph]\n\t"
        "s_cmp_eq_u32 %[ph], 2\n\t"
        "s_cselect_b32 %[delta], %[cm2], %[cp1]\n\t"
        "s_cmp_eq_u32 %[ph], 1\n\t"
        "s_cselect_b32 m0, %[cm2], %[cp1]\n\t"
        "s_add_u32 %[m0n], %[m0n], m0\n\t"
        "s_add_u32 %[soff], %[soff], 8192\n\t"
        "s_cmp_gt_u32 %[cnt], 4\n\t"
        "s_cselect_b32 %[soff], %[oob], %[soff]\n\t"
        "s_add_u32 %[cnt], %[cnt], 1\n\t"
        "s_cmp_lt_u32 %[cnt], 8\n\t"
        "s_cbranch_scc1 1b\n\t"
        MF(R_OB, R_V1, R_P1, R_OB)
        "s_waitcnt vmcnt(0) lgkmcnt(0)\n\ts_nop 15\n\t"
        : "+{v[16:31]}"(oa), "+{v[32:47]}"(ob), [aK] "+&v"(aK), [aV0] "+&v"(aV0), [aV1] "+&v"(aV1), [cnt] "+&s"(cnt), [ph] "+&s"(ph),
          [delta] "+&s"(delta), [m0n] "+&s"(m0n), [soff] "+&s"(soff)
        : "{v[80:95]}"(cneg), "{v[96:99]}"(qa), "{v[100:103]}"(qbf), [voff] "v"(voff), [skv] "s"(skv),
          [cp1] "s"(AT_SLOT), [cm2] "s"(-2 * AT_SLOT), [oob] "s"(OOB)
        : "memory", "scc", "v48", "v49", "v50", "v51", "v52", "v53", "v54", "v55", "v56", "v57", "v58", "v59", "v60", "v61", "v62", "v63", "v64",
          "v65", "v66", "v67", "v68", "v69", "v70", "v71", "v72", "v73", "v74", "v75", "v76", "v77", "v78", "v79", "v104", "v105", "v106",
          "v107", "v108", "v109", "v110", "v111", "v112", "v113", "v114", "v115", "v116", "v117", "v118", "v119", "v120", "v121", "v122", "v123");
  } else {
    ma = -INFINITY; mb = -INFINITY;
    const s16x8 qf[2] = {__builtin_bit_cast(s16x8, qa), __builtin_bit_cast(s16x8, qbf)};
    int slot = 0;
#pragma unroll 1
    for (int c = 0; c < 8; ++c) {
#pragma unroll 1
      for (int jt = 0; jt < 4; ++jt) {
        const unsigned so = (unsigned)(slot * AT_SLOT + jt * AT_TILE);
        const s16x8 kf = lds_frag(aK + so), v0 = lds_frag(aV0 + so), v1 = lds_frag(aV1 + so);
#pragma unroll
        for (int qi = 0; qi < 2; ++qi) {
          f32x16& o = qi ? ob : oa;
          float& m = qi ? mb : ma;
          f32x16 s = MFMA(kf, qf[qi], f32x16{});
          float tm = s[0];
#pragma unroll
          for (int i = 1; i < 16; ++i) tm = fmaxf(tm, s[i]);
          const float mn = fmaxf(m, red_x32<true>(tm));
          const float f = __builtin_amdgcn_exp2f(m - mn);
          m = mn;
#pragma unroll
          for (int i = 0; i < 16; ++i) { o[i] *= f; s[i] = __builtin_amdgcn_exp2f(s[i] - mn); }
          const u32x4 p0 = {pk_bf16(s[0], s[1]), pk_bf16(s[2], s[3]), pk_bf16(s[4], s[5]), pk_bf16(s[6], s[7])};
          const u32x4 p1 = {pk_bf16(s[8], s[9]), pk_bf16(s[10], s[11]), pk_bf16(s[12], s[13]), pk_bf16(s[14], s[15])};
          o = MFMA(v0, __builtin_bit_cast(s16x8, p0), o);
          o = MFMA(v1, __builtin_bit_cast(s16x8, p1), o);
        }
      }
      wait_bar<0>();
      if (c < 6) issue(c + 2, slot == 0 ? 2 : slot - 1);
      slot = slot == 2 ? 0 : slot + 1;
    }
  }
  STAMP(7);
  {
    const unsigned ex = lds0 + AT_EX + w * 2560;
    const f32x16& snd = kh ? oa : ob;
    lds_st<f32x4>(ex + lane * 16, f32x4{snd[0], snd[1], snd[2], snd[3]});
    lds_st<f32x4>(ex + 1024 + lane * 16, f32x4{snd[4], snd[5], snd[6], snd[7]});
    lds_st<f32x2>(ex + 2048 + lane * 8, f32x2{snd[8], kh ? ma : mb});
  }
  lds_bar();
  {
    const unsigned ex = lds0 + AT_EX + (w ^ 4) * 2560;
    const f32x16& own = kh ? ob : oa;
    const float mo = kh ? mb : ma;
    const f32x4 t0 = lds_ld<f32x4>(ex + lane * 16), t1 = lds_ld<f32x4>(ex + 1024 + lane * 16);
    const f32x2 t2 = lds_ld<f32x2>(ex + 2048 + lane * 8);
    const float mm = fmaxf(mo, t2[1]);
    const float f0 = __builtin_amdgcn_exp2f(mo - mm), f1 = __builtin_amdgcn_exp2f(t2[1] - mm);
    const float inv = __builtin_amdgcn_rcpf(own[8] * f0 + t2[0] * f1);
    const float g0 = f0 * inv, g1 = f1 * inv;
    const u32x4 pk = {pk_bf16(own[0] * g0 + t0[0] * g1, own[1] * g0 + t0[1] * g1), pk_bf16(own[2] * g0 + t0[2] * g1, own[3] * g0 + t0[3] * g1),
                      pk_bf16(own[4] * g0 + t1[0] * g1, own[5] * g0 + t1[1] * g1), pk_bf16(own[6] * g0 + t1[2] * g1, own[7] * g0 + t1[3] * g1)};
    st16c(sws, WS_O + (unsigned)(((qt + kh) * 32 + head) * 1024) + voff, pk);
  }
  publish(ws, WS_FO + (qb * 32 + head) * 8, tag);
}

DI float geglu(float g, float u) {
  const float c1 = -2.0f * 0.7978845608028654f * 1.4426950408889634f, c2 = c1 * 0.044715f;
  const float e = __builtin_amdgcn_exp2f(g * __builtin_fmaf(c2 * g, g, c1));
  return g * u * __builtin_amdgcn_rcpf(1.0f + e);
}

DI void ln_phase(const char* x, char* ws, unsigned lds0, int j, int w, unsigned& epoch) {
  const int lane = TID & 63;
  u32x4 xv;
  asm volatile("global_load_dwordx4 %0, %1, %2" : "=v"(xv) : "v"(lane * 16), "s"(x + (8 * j + w) * 1024) : "memory");
  epoch = get_epoch(ws);
  asm volatile("s_waitcnt vmcnt(0)" : "+v"(xv)::"memory");
  float f[8];
#pragma unroll
  for (int i = 0; i < 4; ++i) { f[2 * i] = bf_lo(xv[i]); f[2 * i + 1] = bf_hi(xv[i]); }
  const float mean = red_x32<false>(red32<false>(((f[0] + f[1]) + (f[2] + f[3])) + ((f[4] + f[5]) + (f[6] + f[7])))) * (1.0f / DM);
  float q = 0.f;
#pragma unroll
  for (int i = 0; i < 8; ++i) q = __builtin_fmaf(f[i] - mean, f[i] - mean, q);
  const float var = red_x32<false>(red32<false>(q)) * (1.0f / DM);
  const float m = rbf(mean), dd = rbf(__builtin_amdgcn_sqrtf(rbf(rbf(var) + 1e-5f))), a = __builtin_amdgcn_rcpf(dd);
  if (lane == 0) flag_store(ws, WS_GS + (8 * j + w) * 8, ((u64)~epoch << 32) | __float_as_uint(m) | (__float_as_uint(dd) >> 16));
  u32x4 pk;
#pragma unroll
  for (int i = 0; i < 4; ++i) { const unsigned d = pk_bf16(f[2 * i] - m, f[2 * i + 1] - m); pk[i] = pk_bf16(bf_lo(d) * a, bf_hi(d) * a); }
  lds_st<u32x4>(lds0 + w * 1040 + lane * 16, pk);
  lds_bar();
  const int row = 8 * j + (lane & 7), ch = 8 * w + (lane >> 3);
  pk = lds_ld<u32x4>(lds0 + (lane & 7) * 1040 + ch * 16);
  st16c(make_srd(ws, WS_END), WS_XN + (unsigned)((((row >> 5) * 32 + (ch >> 1)) * 64 + (ch & 1) * 32 + (row & 31)) * 16), pk);
  publish(ws, WS_FL + j * 8, make_tag(epoch));
}

DI void up_phase(const char* wg, const char* wu, char* ws, unsigned lds0, int j, int w, u64 tag STAMP_ARG) {
  const int lane = TID & 63, hh = lane >> 5, tp = w >> 2, ft = w & 3;
  const int xcd = j & 7, i = j >> 3, tb = 4 * (xcd >> 1) + (i & 3), fb = 7 * (xcd & 1) + (i >> 2);
  if (w == 0) poll(ws, lane < 16 ? WS_FL + (tb * 16 + lane) * 8 : WS_FS + j * 8, tag);
  lds_bar();
  STAMP(2);
  const srd_t sx = make_srd(ws + WS_XN, 2u << 20), sg = make_srd(wg, DM * DF * 2), su = make_srd(wu, DM * DF * 2);
  const unsigned voX = lane * 16, soX = (unsigned)(((tb * 4 + (w >> 1)) * 32 + (w & 1)) * 1024);
  const unsigned voW = (unsigned)((4 * w + (lane >> 4)) * (DF * 2) + fb * 256 + (((lane & 15) ^ (((lane >> 4) & 3) << 2)) * 16));
  auto issue = [&](int st, unsigned slot) {
    const unsigned sl = lds0 + slot + (unsigned)w * 1024u;
    dma16c(sl, voX, sx, st < 16 ? soX + st * 2048 : OOB);
    dma16(sl + 8192, voW, sg, st * 32 * DF * 2);
    dma16(sl + 16384, voW, su, st * 32 * DF * 2);
  };
  issue(0, 0); issue(1, 24576); issue(2, 49152);
  const int q = (lane & 15) >> 2;
  const unsigned aX = lds0 + tp * 4096 + lane * 16;
  const unsigned aG = lds0 + 8192 + (unsigned)((8 * hh + q) * 256 + ((ft ^ q) * 64) + ((lane >> 4) & 1) * 32 + (lane & 3) * 8);
  f32x16 ga = {}, gb = {}, ua = {}, ub = {};
  s16x8 xa0, xb0, fg0, fu0, xa1, xb1, fg1, fu1;
#define URD(XA, XB, FG, FU, SL, S) do { XA = lds_frag(aX + (SL) + (S) * 1024); XB = lds_frag(aX + (SL) + 2048 + (S) * 1024); \
    FG = lds_tr_frag(aG + (SL) + (S) * 4096, aG + (SL) + (S) * 4096 + 1024); FU = lds_tr_frag(aG + (SL) + 8192 + (S) * 4096, aG + (SL) + 8192 + (S) * 4096 + 1024); SBAR(); } while (0)
#define UMM(XA, XB, FG, FU) do { ga = MFMA(FG, XA, ga); gb = MFMA(FG, XB, gb); ua = MFMA(FU, XA, ua); ub = MFMA(FU, XB, ub); SBAR(); } while (0)
#define USTAGE(ST, SL, SN) do { URD(xa1, xb1, fg1, fu1, SL, 1); UMM(xa0, xb0, fg0, fu0); wait_bar<3>(); \
    URD(xa0, xb0, fg0, fu0, SN, 0); UMM(xa1, xb1, fg1, fu1); issue((ST) + 3, SL); } while (0)
  wait_bar<6>();
  URD(xa0, xb0, fg0, fu0, 0, 0);
#pragma unroll 1
  for (int st = 0; st < 15; st += 3) { USTAGE(st, 0, 24576); USTAGE(st + 1, 24576, 49152); USTAGE(st + 2, 49152, 0); }
  URD(xa1, xb1, fg1, fu1, 0, 1); UMM(xa0, xb0, fg0, fu0); UMM(xa1, xb1, fg1, fu1);
#undef USTAGE
#undef URD
#undef UMM
  STAMP(3);
  __builtin_amdgcn_s_setprio(3);
  const srd_t sws = make_srd(ws, WS_END);
  const unsigned ho = WS_H + (unsigned)(((tb * 4 + tp * 2) * 112 + fb * 8 + ft * 2) * 1024) + lane * 16;
#pragma unroll
  for (int t = 0; t < 2; ++t) {
    const f32x16& g = t ? gb : ga;
    const f32x16& u = t ? ub : ua;
#pragma unroll
    for (int e = 0; e < 2; ++e) {
      u32x4 pk;
#pragma unroll
      for (int k = 0; k < 4; ++k) pk[k] = pk_bf16(geglu(g[8 * e + 2 * k], u[8 * e + 2 * k]), geglu(g[8 * e + 2 * k + 1], u[8 * e + 2 * k + 1]));
      st16c(sws, ho + t * 112 * 1024 + e * 1024, pk);
    }
  }
  __builtin_amdgcn_s_setprio(0);
  publish(ws, WS_FH + (tb * 16 + fb) * 8, tag);
}

DI void out_phase(const char* x, const char* wd, const char* wo, char* ws, char* out, unsigned lds0, int j, int w, u64 tag STAMP_ARG) {
  const int lane = TID & 63, r = lane & 31, hh = lane >> 5;
  const int xcd = j & 7, i = j >> 3, mb = 4 * xcd + (i & 3), nb = i >> 2;
  const int tl = w & 1, nt = (w >> 1) & 1, kh = w >> 2;
  const unsigned voX = lane * 16, soH = (unsigned)(((2 * mb + (w >> 2)) * 112 + (w & 3)) * 1024);
  const unsigned voW = (unsigned)((8 * w + (lane >> 3)) * 1024 + nb * 128 + (((lane & 7) ^ (((lane >> 4) & 1) << 2)) * 16));
  {
    const srd_t sw = make_srd(wo, DM * DM * 2);
#pragma unroll
    for (int st = 0; st < 8; ++st) dma16(lds0 + st * 8192 + w * 1024, voW, sw, st * 65536);
  }
  const int ocol = 64 * nb + 32 * nt + 16 * kh + 4 * hh, orow = 64 * mb + 32 * tl + r;
  const u32x2 xr0 = *(const u32x2*)(x + orow * 1024 + ocol * 2), xr1 = *(const u32x2*)(x + orow * 1024 + ocol * 2 + 16);
  if (w == 0) poll(ws, WS_FH + ((mb >> 1) * 16 + (lane < 14 ? lane : 0)) * 8, tag);
  wait_bar<0>();
  const int q = (lane & 15) >> 2;
  const unsigned aX = lds0 + tl * 4096 + kh * 2048 + lane * 16;
  const unsigned aW = lds0 + (unsigned)((32 * kh + 4 * hh + q) * 128 + ((nt ^ (q >> 1)) * 64) + ((lane >> 4) & 1) * 32 + (lane & 3) * 8);
  s16x8 wf[16];
#pragma unroll
  for (int k = 0; k < 16; ++k) wf[k] = lds_tr_frag(aW + (k >> 1) * 8192 + (k & 1) * 2048, aW + (k >> 1) * 8192 + (k & 1) * 2048 + 1024);
  lds_bar();
  STAMP(5);
  const srd_t sh = make_srd(ws + WS_H, 64u * 112u * 1024u), sd = make_srd(wd, DF * DM * 2);
  auto issue = [&](int st, unsigned slot) {
    const unsigned sl = lds0 + slot + (unsigned)w * 1024u;
    dma16c(sl, voX, sh, st < 28 ? soH + st * 4096 : OOB);
    dma16(sl + 8192, voW, sd, st * 65536);
  };
#pragma unroll
  for (int st = 0; st < 4; ++st) issue(st, st * 16384);
  f32x16 acc = {};
  {
    s16x8 x0, w0, x1, w1;
#define DRD(X, W, SL, E) do { X = lds_frag(aX + (SL) + (E) * 1024); W = lds_tr_frag(aW + 8192 + (SL) + (E) * 2048, aW + 8192 + (SL) + (E) * 2048 + 1024); SBAR(); } while (0)
#define DSTAGE(ST, SL, SN) do { DRD(x1, w1, SL, 1); acc = MFMA(w0, x0, acc); SBAR(); wait_bar<4>(); \
    DRD(x0, w0, SN, 0); acc = MFMA(w1, x1, acc); SBAR(); issue((ST) + 4, SL); } while (0)
    wait_bar<6>();
    DRD(x0, w0, 0, 0);
#pragma unroll 1
    for (int st = 0; st < 28; st += 4) { DSTAGE(st, 0, 16384); DSTAGE(st + 1, 16384, 32768); DSTAGE(st + 2, 32768, 49152); DSTAGE(st + 3, 49152, 0); }
#undef DSTAGE
#undef DRD
  }
  STAMP(7);
  if (w == 0) poll(ws, WS_FO + ((mb >> 2) * 32 + r) * 8, tag);
  wait_bar<0>();
  STAMP(8);
  {
    const srd_t so = make_srd(ws + WS_O, 2u << 20);
    const unsigned soO = (unsigned)(((2 * mb + (w >> 2)) * 32 + (w & 3)) * 1024);
#pragma unroll
    for (int st = 0; st < 8; ++st) dma16c(lds0 + st * 8192 + w * 1024, voX, so, soO + st * 4096);
  }
#pragma unroll
  for (int g = 0; g < 4; ++g) {
    if (g == 0) wait_bar<6>(); else if (g == 1) wait_bar<4>(); else if (g == 2) wait_bar<2>(); else wait_bar<0>();
    s16x8 xf[4];
#pragma unroll
    for (int k = 0; k < 4; ++k) xf[k] = lds_frag(aX + (2 * g + (k >> 1)) * 8192 + (k & 1) * 1024);
#pragma unroll
    for (int k = 0; k < 4; ++k) acc = MFMA(wf[4 * g + k], xf[k], acc);
  }
  STAMP(9);
  {
    const unsigned ex = lds0 + 65536 + w * 2048 + lane * 16;
    lds_st<f32x4>(ex, kh ? f32x4{acc[0], acc[1], acc[2], acc[3]} : f32x4{acc[8], acc[9], acc[10], acc[11]});
    lds_st<f32x4>(ex + 1024, kh ? f32x4{acc[4], acc[5], acc[6], acc[7]} : f32x4{acc[12], acc[13], acc[14], acc[15]});
  }
  lds_bar();
  {
    const unsigned ex = lds0 + 65536 + (w ^ 4) * 2048 + lane * 16;
    const f32x4 t0 = lds_ld<f32x4>(ex), t1 = lds_ld<f32x4>(ex + 1024);
    float v[8];
#pragma unroll
    for (int k = 0; k < 4; ++k) { v[k] = (kh ? acc[8 + k] : acc[k]) + t0[k]; v[4 + k] = (kh ? acc[12 + k] : acc[4 + k]) + t1[k]; }
    const u32x2 o0 = {pk_bf16(v[0] + bf_lo(xr0[0]), v[1] + bf_hi(xr0[0])), pk_bf16(v[2] + bf_lo(xr0[1]), v[3] + bf_hi(xr0[1]))};
    const u32x2 o1 = {pk_bf16(v[4] + bf_lo(xr1[0]), v[5] + bf_hi(xr1[0])), pk_bf16(v[6] + bf_lo(xr1[1]), v[7] + bf_hi(xr1[1]))};
    *(u32x2*)(out + orow * 1024 + ocol * 2) = o0;
    *(u32x2*)(out + orow * 1024 + ocol * 2 + 16) = o1;
  }
}

__global__ __launch_bounds__(512, 4) void fused_block(const char* __restrict__ x, const char* __restrict__ wqkv, const char* __restrict__ wo,
                                                      const char* __restrict__ wg, const char* __restrict__ wu, const char* __restrict__ wd,
                                                      char* ws, char* __restrict__ out) {
  extern __shared__ __attribute__((aligned(16))) char lds[];
  const unsigned lds0 = (unsigned)(uintptr_t)(LDSP char*)lds;
  STAMP_DECL; STAMP(0);
  int w = __builtin_amdgcn_readfirstlane(TID >> 6);
  asm volatile("s_nop 4" : "+s"(w));
  unsigned epoch;
  if (BID >= 256) {
    const int b = BID - 256;
    __builtin_amdgcn_s_setprio(2);
    const int xcd = b & 7, i = b >> 3, rb = 4 * (xcd >> 1) + (i & 3), cb = 8 * (xcd & 1) + (i >> 2);
    if (w >= 4) qkv_loader(x, wqkv, ws, lds0, b, rb, cb, w, epoch STAMP_PASS);
    else if (cb < 10) qkv_wave<0>(ws, lds0, rb, cb, w, epoch STAMP_PASS);
    else if (cb == 10) qkv_wave<1>(ws, lds0, rb, cb, w, epoch STAMP_PASS);
    else qkv_wave<3>(ws, lds0, rb, cb, w, epoch STAMP_PASS);
    const u64 tag = make_tag(epoch);
    publish(ws, WS_FQ + (cb * 16 + rb) * 8, tag);
    if (HINT_AT == 1 && TID == 0) flag_store(ws, WS_FS + b * 8, tag);
    STAMP(4);
    attn_phase(ws, lds0, b, w, tag STAMP_PASS);
  } else {
    const int j = BID;
    ln_phase(x, ws, lds0, j, w, epoch);
    const u64 tag = make_tag(epoch);
    STAMP(1);
    if (j < 224) {
      up_phase(wg, wu, ws, lds0, j, w, tag STAMP_PASS);
    } else if (j == 255 && w == 1) {
      const int lane = TID & 63;
#pragma unroll 1
      for (int k = 0; k < 8; ++k) poll(ws, WS_FQ + (k * 64 + lane) * 8, tag);
      if (lane == 0) __hip_atomic_store((gu32*)(ws + WS_EP), epoch, __ATOMIC_RELAXED, __HIP_MEMORY_SCOPE_AGENT);
    }
    STAMP(4);
    out_phase(x, wd, wo, ws, out, lds0, j, w, tag STAMP_PASS);
  }
  STAMP_FLUSH(ws);
}

extern "C" void kernel_launch(void* const* d_in, const int* in_sizes, int n_in, void* d_out, int out_size, void* d_ws, size_t ws_size,
                              hipStream_t stream) {
  (void)hipFuncSetAttribute((const void*)fused_block, hipFuncAttributeMaxDynamicSharedMemorySize, LDS_BYTES);
  fused_block<<<512, 512, LDS_BYTES, stream>>>((const char*)d_in[0], (const char*)d_in[1], (const char*)d_in[2], (const char*)d_in[4],
                                               (const char*)d_in[5], (const char*)d_in[6], (char*)d_ws, (char*)d_out);
}

#if defined(__HIP_DEVICE_COMPILE__)
#pragma clang attribute pop
#endif
```

```cpp
#if defined(__HIP_DEVICE_COMPILE__)
#pragma clang attribute push(__attribute__((target("no-packed-fp32-ops"))), apply_to = function)
#endif
#include <hip/hip_runtime.h>
#include <stdint.h>

typedef float f32x16 __attribute__((ext_vector_type(16)));
typedef float f32x4 __attribute__((ext_vector_type(4)));
typedef float f32x2 __attribute__((ext_vector_type(2)));
typedef short s16x8 __attribute__((ext_vector_type(8)));
typedef short s16x4 __attribute__((ext_vector_type(4)));
typedef __bf16 bf16x2 __attribute__((ext_vector_type(2)));
typedef unsigned u32x4 __attribute__((ext_vector_type(4)));
typedef unsigned u32x2 __attribute__((ext_vector_type(2)));
typedef unsigned long long u64;
typedef __attribute__((address_space(1))) u64 gu64;
typedef __attribute__((address_space(1))) unsigned gu32;
typedef __amdgpu_buffer_rsrc_t srd_t;
#define LDSP __attribute__((address_space(3)))
#define DI __device__ __forceinline__
#define MFMA(a, b, c) __builtin_amdgcn_mfma_f32_32x32x16_bf16((a), (b), (c), 0, 0, 0)
#define SBAR() __builtin_amdgcn_sched_barrier(0)
#define TID ((int)__builtin_amdgcn_workitem_id_x())
#define BID ((int)__builtin_amdgcn_workgroup_id_x())
#define POLL_SLEEP 4
#define CODEPF 1
#define KNAME "fused_block"
#define ROTQ 0
#define ROTU 0
#define ROTD 1
#define PREFETCH 0
#define HINT_AT 0
#define HINT_ST 3

constexpr int SEQ = 2048, DM = 512, DF = 1792;
constexpr unsigned WS_XN = 0;
constexpr unsigned WS_Q = WS_XN + (2u << 20);
constexpr unsigned WS_KV = WS_Q + (2u << 20);
constexpr unsigned WS_O = WS_KV + (4u << 20);
constexpr unsigned WS_H = WS_O + (2u << 20);
constexpr unsigned WS_KN = WS_H + 64u * 112u * 1024u;
constexpr unsigned WS_GS = WS_KN + 16384;
constexpr unsigned WS_FQ = WS_GS + 16384;
constexpr unsigned WS_FL = WS_FQ + 2048;
constexpr unsigned WS_FH = WS_FL + 2048;
constexpr unsigned WS_FO = WS_FH + 2048;
constexpr unsigned WS_FS = WS_FO + 2048;
constexpr unsigned WS_EP = WS_FS + 2048;
constexpr unsigned WS_END = WS_EP + 256;
constexpr float QSCALE = 0.25f * 1.4426950408889634f;
constexpr float BOUND_LIMIT = 48.0f;
constexpr unsigned OOB = 0x40000000u;
constexpr int LDS_BYTES = 81920;

DI bool all_lanes(bool c) { return __builtin_amdgcn_ballot_w64(!c) == 0; }
DI srd_t make_srd(const void* p, unsigned bytes) { return __builtin_amdgcn_make_buffer_rsrc((void*)p, (short)0, (int)bytes, 0x00020000); }
DI unsigned pk_bf16(float lo, float hi) { f32x2 v = {lo, hi}; bf16x2 b = __builtin_convertvector(v, bf16x2); return __builtin_bit_cast(unsigned, b); }
DI float rbf(float v) { return __uint_as_float(pk_bf16(v, v) << 16); }
DI float bf_lo(unsigned u) { return __uint_as_float(u << 16); }
DI float bf_hi(unsigned u) { return __uint_as_float(u & 0xffff0000u); }
template <bool MAX> DI float comb(float a, float b) { return MAX ? fmaxf(a, b) : a + b; }
template <bool MAX, int CTRL> DI float red_dpp(float v) { return comb<MAX>(v, __int_as_float(__builtin_amdgcn_update_dpp(0, __float_as_int(v), CTRL, 0xf, 0xf, true))); }
template <bool MAX> DI float red_x16(float v) { auto r = __builtin_amdgcn_permlane16_swap(__float_as_uint(v), __float_as_uint(v), false, false); return comb<MAX>(__uint_as_float(r[0]), __uint_as_float(r[1])); }
template <bool MAX> DI float red_x32(float v) { auto r = __builtin_amdgcn_permlane32_swap(__float_as_uint(v), __float_as_uint(v), false, false); return comb<MAX>(__uint_as_float(r[0]), __uint_as_float(r[1])); }
template <bool MAX> DI float red32(float v) {
  v = red_dpp<MAX, 0xB1>(v); v = red_dpp<MAX, 0x4E>(v); v = red_dpp<MAX, 0x141>(v); v = red_dpp<MAX, 0x140>(v); return red_x16<MAX>(v);
}
DI void dma16(unsigned lds, unsigned voff, srd_t srd, unsigned soff) {
  asm volatile("s_mov_b32 m0, %0\n\ts_nop 0\n\tbuffer_load_dwordx4 %1, %2, %3 offen lds" ::"s"(lds), "v"(voff), "s"(srd), "s"(soff) : "memory");
}
DI void dma16c(unsigned lds, unsigned voff, srd_t srd, unsigned soff) {
  asm volatile("s_mov_b32 m0, %0\n\ts_nop 0\n\tbuffer_load_dwordx4 %1, %2, %3 offen sc1 lds" ::"s"(lds), "v"(voff), "s"(srd), "s"(soff) : "memory");
}
template <int N> DI void wait_bar() { asm volatile("s_waitcnt vmcnt(%0) lgkmcnt(0)\n\ts_barrier" ::"i"(N) : "memory"); }
DI void lds_bar() { asm volatile("s_waitcnt lgkmcnt(0)\n\ts_barrier" ::: "memory"); }
DI s16x8 lds_frag(unsigned a) { return *(LDSP const s16x8*)(uintptr_t)a; }
DI s16x8 lds_tr_frag(unsigned a0, unsigned a1) {
  s16x4 a = __builtin_amdgcn_ds_read_tr16_b64_v4i16((LDSP s16x4*)(uintptr_t)a0), b = __builtin_amdgcn_ds_read_tr16_b64_v4i16((LDSP s16x4*)(uintptr_t)a1);
  return __builtin_shufflevector(a, b, 0, 1, 2, 3, 4, 5, 6, 7);
}
template <class T> DI T lds_ld(unsigned a) { return *(LDSP const T*)(uintptr_t)a; }
template <class T> DI void lds_st(unsigned a, T v) { *(LDSP T*)(uintptr_t)a = v; }
DI void st16c(srd_t srd, unsigned voff, u32x4 v) { __builtin_amdgcn_raw_buffer_store_b128(v, srd, voff, 0, 16); }
DI u64 make_tag(unsigned e) { return ((u64)(e ^ 0x5bd1e995u) << 32) | e; }
DI unsigned get_epoch(const char* ws) {
  unsigned e;
  asm volatile("s_load_dword %0, %1, %2\n\ts_waitcnt lgkmcnt(0)" : "=s"(e) : "s"(ws), "s"(WS_EP) : "memory");
  return e + 1u;
}
DI void flag_store(char* ws, unsigned off, u64 tag) { __hip_atomic_store((gu64*)(ws + off), tag, __ATOMIC_RELAXED, __HIP_MEMORY_SCOPE_AGENT); }
DI void publish(char* ws, unsigned off, u64 tag) { wait_bar<0>(); if (TID == 0) flag_store(ws, off, tag); }
DI void poll(char* ws, unsigned off, u64 tag) {
#pragma unroll 1
  for (int it = 0; it < (1 << 20); ++it) {
    const u64 v = __hip_atomic_load((gu64*)(ws + off), __ATOMIC_RELAXED, __HIP_MEMORY_SCOPE_AGENT);
    if (all_lanes(v == tag)) break;
    __builtin_amdgcn_s_sleep(POLL_SLEEP);
  }
}

#define STAMP_DECL do {} while (0)
#define STAMP(i) do {} while (0)
#define STAMP_FLUSH(ws) do {} while (0)
#define STAMP_ARG
#define STAMP_PASS

constexpr unsigned Q_W = 49152, Q_STAT = Q_W + 5 * 6144, Q_CS = Q_STAT + 1024;

template <int PART>
DI void qkv_loader(const char* x, const char* wqkv, char* ws, unsigned lds0, int b, int rb, int cb, int w, unsigned& epoch STAMP_ARG) {
  const int lane = TID & 63, L = w - 4;
  const srd_t sx = make_srd(x, SEQ * DM * 2), sw = make_srd(wqkv, DM * 3 * DM * 2);
  const unsigned voX0 = (unsigned)((rb * 128 + 32 * L + (lane >> 3)) * 1024 + (((lane & 7) ^ (lane >> 4)) * 16));
  const unsigned voX1 = (unsigned)((rb * 128 + 32 * L + 8 + (lane >> 3)) * 1024 + (((lane & 7) ^ (4 + (lane >> 4))) * 16)) - 1024u;
  const unsigned voW0 = (unsigned)((8 * L + lane / 12) * 3072 + cb * 192 + (lane % 12) * 16), voW1 = voW0 + 4 * 3072 - 768;
  const unsigned rot = ROTQ ? (unsigned)(cb + 2 * rb) : 0u;
  auto group = [&](unsigned hslot, unsigned hs, unsigned xslot, unsigned st, int part) {
    asm volatile("s_mov_b32 m0, %0\n\ts_mov_b32 exec_hi, 0xffff\n\tbuffer_load_dwordx4 %1, %3, %4 offen lds\n\tbuffer_load_dwordx4 %2, %3, %4 offen offset:768 lds\n\t"
                 "s_mov_b32 exec_hi, -1"
                 ::"s"(lds0 + Q_W + hslot + L * 1536), "v"(voW0), "v"(voW1), "s"(sw), "s"(((hs + 2 * rot) & 15) * 98304) : "memory");
    asm volatile("s_mov_b32 m0, %0\n\ts_nop 0\n\tbuffer_load_dwordx4 %1, %3, %4 offen lds\n\tbuffer_load_dwordx4 %2, %3, %4 offen offset:1024 lds"
                 ::"s"(lds0 + xslot + L * 4096 + part * 2048), "v"(voX0), "v"(voX1), "s"(sx), "s"(((st + rot) & 7) * 128 + part * 16384) : "memory");
  };
  if (PART == 0) {
    STAMP(8);
    group(0, 0, 0, 0, 0); group(6144, 1, 0, 0, 1); group(2 * 6144, 2, 16384, 1, 0); group(3 * 6144, 3, 16384, 1, 1);
    STAMP(10);
    return;
  }
  epoch = get_epoch(ws);
  STAMP(1);
#pragma unroll
  for (int st = 0; st < 6; ++st) {
    wait_bar<8>();
    group(((2 * st + 4) % 5) * 6144, 2 * st + 4, ((st + 2) % 3) * 16384, st + 2, 0);
    wait_bar<12>();
    group(((2 * st + 5) % 5) * 6144, 2 * st + 5, ((st + 2) % 3) * 16384, st + 2, 1);
    if (HINT_AT == 0 && st == HINT_ST && TID == 448) flag_store(ws, WS_FS + b * 8, make_tag(epoch));
  }
  wait_bar<8>();
  const srd_t sws = make_srd(ws, WS_END);
  const unsigned go = WS_GS + rb * 1024 + lane * 16;
  u32x4 gs;
  asm volatile("buffer_load_dwordx4 %0, %1, %2, 0 offen sc1" : "=v"(gs) : "v"(go), "s"(sws) : "memory");
  wait_bar<9>();
  asm volatile("s_waitcnt vmcnt(0)\n\ts_barrier\n\ts_barrier" : "+v"(gs)::"memory");
  if (L == 0) {
#pragma unroll 1
    for (int it = 0; it < (1 << 16) && !all_lanes(gs[1] == ~epoch && gs[3] == ~epoch); ++it) gs = __builtin_amdgcn_raw_buffer_load_b128(sws, go, 0, 16);
    lds_st<f32x4>(lds0 + Q_STAT + lane * 16, f32x4{bf_hi(gs[0]), __builtin_amdgcn_rcpf(bf_lo(gs[0])), bf_hi(gs[2]), __builtin_amdgcn_rcpf(bf_lo(gs[2]))});
  }
  STAMP(3);
  lds_bar();
}

template <int NV>
DI void qkv_wave(char* ws, unsigned lds0, int rb, int cb, int rt, unsigned& epoch STAMP_ARG) {
  const int lane = TID & 63, r = lane & 31, hh = lane >> 5;
  epoch = get_epoch(ws);
  const int swz = (r >> 1) & 7, sw = rt & 1;
  unsigned aX[4];
#pragma unroll
  for (int s = 0; s < 4; ++s) aX[s] = lds0 + (unsigned)((32 * rt + r) * 128 + (((2 * (s ^ sw) + hh) ^ swz) * 16));
  const unsigned aWb = lds0 + Q_W + (unsigned)((8 * hh + ((lane & 15) >> 2)) * 192 + ((lane >> 4) & 1) * 32 + (lane & 3) * 8);
  const unsigned aW[2] = {aWb + sw * 3072, aWb + (sw ^ 1) * 3072};
  f32x16 acc[3] = {};
  f32x4 csum[3] = {};
  s16x4 ones = {0x3f80, 0x3f80, 0x3f80, 0x3f80};
  asm volatile("" : "+v"(ones));
  s16x8 xf[3];
  s16x4 wl[3][3], wh[3][3];
#define TRD(A) __builtin_amdgcn_ds_read_tr16_b64_v4i16((LDSP s16x4*)(uintptr_t)(A))
#define QRX(F, S) xf[F] = lds_frag(aX[S] + xs)
#define QRL(F, J) wl[F][J] = TRD(aW[(F) != 0] + hr + 64 * (J))
#define QRH(F, J) wh[F][J] = TRD(aW[(F) != 0] + hr + 768 + 64 * (J))
#define QM(F, J) do { const s16x8 w_ = __builtin_shufflevector(wl[F][J], wh[F][J], 0, 1, 2, 3, 4, 5, 6, 7); \
    acc[J] = (J) >= 3 - NV ? MFMA(xf[F], w_, acc[J]) : MFMA(w_, xf[F], acc[J]); } while (0)
#define QC(J) do { csum[J] = __builtin_amdgcn_mfma_f32_4x4x4bf16_1k(ones, wl[0][J], csum[J], 0, 0, 0); } while (0)
#define QD(J) do { csum[J] = __builtin_amdgcn_mfma_f32_4x4x4bf16_1k(ones, wh[0][J], csum[J], 0, 0, 0); } while (0)
#define QHALF(H, S, N, P) do { const unsigned xs = (((H) >> 1) % 3) * 16384, hr = ((H) % 5) * 6144; lds_bar(); SBAR(); \
    if ((H) > 0) { QC(0); QC(1); QC(2); QD(0); QD(1); QD(2); SBAR(); QM(0, 0); SBAR(); } QRX(N, (S) + 1); QRL(N, 0); QRH(N, 0); QRL(0, 0); SBAR(); \
    if ((H) > 0) { QM(0, 1); SBAR(); } QRH(0, 0); QRL(N, 1); QRH(N, 1); QRL(0, 1); SBAR(); \
    if ((H) > 0) { QM(0, 2); SBAR(); } QRH(0, 1); QRX(0, S); QRL(0, 2); SBAR(); \
    if ((H) > 0) { QM(P, 0); SBAR(); } QRH(0, 2); QRL(N, 2); QRH(N, 2); SBAR(); \
    if ((H) > 0) { QM(P, 1); QM(P, 2); SBAR(); } } while (0)
  STAMP(2);
#pragma unroll
  for (int st = 0; st < 8; ++st) {
    QHALF(2 * st, 0, 1, 2);
    QHALF(2 * st + 1, 2, 2, 1);
    if (st == 0) STAMP(8);
    if (st == 3) STAMP(9);
    if (st == 5) STAMP(10);
  }
  QC(0); QC(1); QC(2); QD(0); QD(1); QD(2);
  QM(0, 0); QM(0, 1); QM(0, 2); QM(2, 0); QM(2, 1); QM(2, 2);
#undef QD
#undef QHALF
#undef QC
#undef QM
#undef QRL
#undef QRH
#undef QRX
#undef TRD
  if (rt < 2) {
#pragma unroll
    for (int j = 0; j < 3; ++j) { const float c = red_x32<false>(csum[j][0]); if (hh == 0) lds_st<float>(lds0 + Q_CS + rt * 384 + (32 * j + r) * 4, c); }
  }
  STAMP(3);
  lds_bar();
  const srd_t sws = make_srd(ws, WS_END);
  const int tt = rb * 4 + rt;
  const f32x2 ma = lds_ld<f32x2>(lds0 + Q_STAT + (32 * rt + r) * 8);
#pragma unroll
  for (int j = 0; j < 3; ++j) {
    const int ct = 3 * cb + j;
    if (j < 3 - NV) {
      const bool isq = ct < 16;
      const float a = ma[1] * (isq ? QSCALE : 1.0f), nm = -ma[0];
      const unsigned obase = (isq ? WS_Q + (unsigned)((2 * ct * 64 + tt) * 1024) : WS_KV + (unsigned)((2 * (ct - 16) * 64 + tt) * 2048)) + lane * 16;
      float n2[2];
#pragma unroll
      for (int e = 0; e < 2; ++e) {
        const unsigned ca = lds0 + Q_CS + (32 * j + 16 * e + 4 * hh) * 4;
        const f32x4 c0 = lds_ld<f32x4>(ca) + lds_ld<f32x4>(ca + 384), c1 = lds_ld<f32x4>(ca + 32) + lds_ld<f32x4>(ca + 32 + 384);
        float o[8], q2 = 0.f;
#pragma unroll
        for (int i = 0; i < 8; ++i) { o[i] = a * __builtin_fmaf(nm, i < 4 ? c0[i] : c1[i - 4], acc[j][8 * e + i]); q2 = __builtin_fmaf(o[i], o[i], q2); }
        st16c(sws, obase + e * (isq ? 65536u : 131072u), u32x4{pk_bf16(o[0], o[1]), pk_bf16(o[2], o[3]), pk_bf16(o[4], o[5]), pk_bf16(o[6], o[7])});
        n2[e] = q2;
      }
      auto sw2 = __builtin_amdgcn_permlane32_swap(__float_as_uint(n2[0]), __float_as_uint(n2[1]), false, false);
      const float kn = red32<true>(__uint_as_float(sw2[0]) + __uint_as_float(sw2[1]));
      if (r == 0) __hip_atomic_store((gu32*)(ws + WS_KN + ((2 * ct + hh) * 64 + tt) * 4), __float_as_uint(kn), __ATOMIC_RELAXED, __HIP_MEMORY_SCOPE_AGENT);
    } else {
      const float c = lds_ld<float>(lds0 + Q_CS + (32 * j + r) * 4) + lds_ld<float>(lds0 + Q_CS + 384 + (32 * j + r) * 4);
      const unsigned obase = WS_KV + (unsigned)(((2 * (ct - 32) + (r >> 4)) * 64 + tt) * 2048 + 1024 + hh * 256 + (r & 15) * 16);
#pragma unroll
      for (int e = 0; e < 2; ++e) {
        u32x4 pk;
#pragma unroll
        for (int g = 0; g < 2; ++g) {
          const unsigned sa = lds0 + Q_STAT + (32 * rt + 16 * e + 8 * g + 4 * hh) * 8;
          const f32x4 m0 = lds_ld<f32x4>(sa), m1 = lds_ld<f32x4>(sa + 16);
          const int i0 = 8 * e + 4 * g;
          pk[2 * g] = pk_bf16(m0[1] * __builtin_fmaf(-m0[0], c, acc[j][i0]), m0[3] * __builtin_fmaf(-m0[2], c, acc[j][i0 + 1]));
          pk[2 * g + 1] = pk_bf16(m1[1] * __builtin_fmaf(-m1[0], c, acc[j][i0 + 2]), m1[3] * __builtin_fmaf(-m1[2], c, acc[j][i0 + 3]));
        }
        st16c(sws, obase + e * 512, pk);
      }
    }
  }
}

constexpr int AT_TILE = 2080, AT_SLOT = 8 * AT_TILE, AT_EX = 3 * AT_SLOT;

#define EXP1(r) "v_exp_f32 v" #r ", v" #r "\n\t"
#define CVT1(d, a, b) "v_cvt_pk_bf16_f32 v" #d ", v" #a ", v" #b "\n\t"
#define MF(d, a, b, c) "v_mfma_f32_32x32x16_bf16 " d ", " a ", " b ", " c "\n\t"
#define R_OA "v[16:31]"
#define R_OB "v[32:47]"
#define R_SA "v[48:63]"
#define R_SB "v[64:79]"
#define R_C "v[80:95]"
#define R_QA "v[96:99]"
#define R_QB "v[100:103]"
#define R_K "v[104:107]"
#define R_V0 "v[108:111]"
#define R_V1 "v[112:115]"
#define R_P0 "v[116:119]"
#define R_P1 "v[120:123]"
#define PRHI ""
#define PRLO ""
#define XBAR "s_waitcnt vmcnt(0)\n\ts_barrier\n\t"
#define XDMA(x) x
#define XLDS(x) x
#define XALIGN ".p2align 6\n\t"
#define UNIT_A(PRE, KOFF, VOFF)                                                                                      \
  PRHI MF(R_SB, R_K, R_QB, R_C) EXP1(48) PRE XLDS("ds_read_b128 " R_K ", %[aK] offset:" KOFF "\n\t")                       \
  EXP1(49) EXP1(50) EXP1(51) EXP1(52) MF(R_OB, R_V1, R_P1, R_OB) EXP1(53)                                            \
  XLDS("ds_read_b128 " R_V1 ", %[aV1] offset:" VOFF "\n\t") EXP1(54) EXP1(55) EXP1(56)                                     \
  CVT1(116, 48, 49) CVT1(117, 50, 51) CVT1(118, 52, 53) CVT1(119, 54, 55) EXP1(57)                                   \
  "s_waitcnt lgkmcnt(2)\n\t" EXP1(58) PRLO MF(R_OA, R_V0, R_P0, R_OA) EXP1(59) EXP1(60) EXP1(61) EXP1(62)            \
  "s_waitcnt lgkmcnt(0)\n\t" EXP1(63)                                                                                \
  CVT1(120, 56, 57) CVT1(121, 58, 59) CVT1(122, 60, 61) CVT1(123, 62, 63)
#define UNIT_B(PRE, VOFF)                                                                                            \
  PRHI MF(R_SA, R_K, R_QA, R_C) EXP1(64) EXP1(65) EXP1(66) EXP1(67) EXP1(68) MF(R_OA, R_V1, R_P1, R_OA)              \
  EXP1(69) EXP1(70) EXP1(71) EXP1(72)                                                                                \
  CVT1(116, 64, 65) CVT1(117, 66, 67) CVT1(118, 68, 69) CVT1(119, 70, 71) EXP1(73) EXP1(74)                          \
  PRLO MF(R_OB, R_V0, R_P0, R_OB) EXP1(75) PRE XLDS("ds_read_b128 " R_V0 ", %[aV0] offset:" VOFF "\n\t")                   \
  EXP1(76) EXP1(77) EXP1(78) EXP1(79)                                                                                \
  CVT1(120, 72, 73) CVT1(121, 74, 75) CVT1(122, 76, 77) CVT1(123, 78, 79)

DI void attn_phase(char* ws, unsigned lds0, int b, int w, u64 tag STAMP_ARG) {
  const int lane = TID & 63, r = lane & 31, hh = lane >> 5, qp = w & 3, kh = w >> 2;
  const int head = (b & 7) * 4 + ((b >> 3) & 3), qb = b >> 5;
  if (TID >= 64 && TID < 112) {
    const unsigned i = TID - 64, v = (i & 1) ? 0u : 0x3f803f80u;
    lds_st<u32x4>(lds0 + (i >> 1) * AT_TILE + 2048 + (i & 1) * 16, u32x4{v, v, v, v});
  }
  if (w == 0) {
    const int hp = head >> 1;
    const int f = lane < 16 ? ((16 + hp) / 3) * 16 + lane : lane < 32 ? ((32 + hp) / 3) * 16 + (lane - 16) : (hp / 3) * 16 + 2 * qb + (lane & 1);
    poll(ws, WS_FQ + f * 8, tag);
  }
  lds_bar();
  if (HINT_AT == 2 && TID == 0) flag_store(ws, WS_FS + b * 8, tag);
  STAMP(5);
  const srd_t skv = make_srd(ws + WS_KV + head * 131072, 131072), sws = make_srd(ws, WS_END);
  const unsigned voff = lane * 16, sbase = (unsigned)((32 * kh + qp) * 2048), mbase = lds0 + (unsigned)(w * AT_TILE);
  auto issue = [&](int c, int slot) {
    asm volatile("s_mov_b32 m0, %0\n\ts_nop 0\n\tbuffer_load_dwordx4 %1, %2, %3 offen sc1 lds\n\tbuffer_load_dwordx4 %1, %2, %3 offen offset:1024 sc1 lds"
                 ::"s"(mbase + (unsigned)(slot * AT_SLOT)), "v"(voff), "s"(skv), "s"(sbase + (unsigned)(c * 8192)) : "memory");
  };
  const int qt = qb * 8 + qp * 2;
  const u32x4 qa = __builtin_amdgcn_raw_buffer_load_b128(sws, voff, WS_Q + (head * 64 + qt) * 1024, 16);
  const u32x4 qbf = __builtin_amdgcn_raw_buffer_load_b128(sws, voff, WS_Q + (head * 64 + qt + 1) * 1024, 16);
  const float kn = __uint_as_float(__builtin_amdgcn_raw_buffer_load_b32(sws, lane * 4, WS_KN + (32 + head) * 256, 16));
  const u32x2 qn = __builtin_amdgcn_raw_buffer_load_b64(sws, 0, WS_KN + head * 256 + qt * 4, 16);
  issue(0, 0);
  const float bound = __builtin_amdgcn_sqrtf(fmaxf(__uint_as_float(qn[0]), __uint_as_float(qn[1])) * red_x32<true>(red32<true>(kn))) * 1.01f;
  const bool safe = all_lanes(bound <= BOUND_LIMIT);
  issue(1, 1);
  unsigned aK = lds0 + (unsigned)(kh * 4 * AT_TILE + lane * 16);
  const unsigned cst = (unsigned)(2048 + ((r == 16 || r == 20) ? 0 : 16));
  unsigned aV0 = lds0 + (unsigned)(kh * 4 * AT_TILE) + (r < 16 ? (unsigned)(1024 + hh * 256 + r * 16) : cst);
  unsigned aV1 = lds0 + (unsigned)(kh * 4 * AT_TILE) + (r < 16 ? (unsigned)(1536 + hh * 256 + r * 16) : cst);
  f32x16 oa = {}, ob = {};
  float ma = bound, mb = bound;
  wait_bar<2>();
  STAMP(6);
  if (safe) {
    f32x16 cneg;
#pragma unroll
    for (int i = 0; i < 16; ++i) cneg[i] = -bound;
    unsigned cnt = 0, delta = AT_SLOT, m0n = mbase + 2 * AT_SLOT, soff = sbase + 2 * 8192, ph = 0;
    asm volatile(
        "ds_read_b128 " R_K ", %[aK]\n\t"
        "ds_read_b128 " R_V0 ", %[aV0]\n\t"
        "v_mov_b32 v112, 0\n\tv_mov_b32 v113, 0\n\tv_mov_b32 v114, 0\n\tv_mov_b32 v115, 0\n\t"
        "v_mov_b32 v120, 0\n\tv_mov_b32 v121, 0\n\tv_mov_b32 v122, 0\n\tv_mov_b32 v123, 0\n\t"
        "s_waitcnt lgkmcnt(0)\n\t" MF(R_SA, R_K, R_QA, R_C)
        "s_nop 15\n\t"
        XALIGN
        "1:\n\t"
        UNIT_A("", "2080", "0") UNIT_B("", "2080")
        UNIT_A("", "4160", "2080") UNIT_B("", "4160")
        UNIT_A("", "6240", "4160") UNIT_B("", "6240")
        UNIT_A(XBAR "v_add_u32 %[aK], %[delta], %[aK]\n\t", "0", "6240")
        "s_mov_b32 m0, %[m0n]\n\t"
        UNIT_B(XDMA("buffer_load_dwordx4 %[voff], %[skv], %[soff] offen sc1 lds\n\t")
               "v_add_u32 %[aV0], %[delta], %[aV0]\n\t"
               XDMA("buffer_load_dwordx4 %[voff], %[skv], %[soff] offen offset:1024 sc1 lds\n\t"), "0")
        "v_add_u32 %[aV1], %[delta], %[aV1]\n\t"
        "s_add_u32 %[ph], %[ph], 1\n\t"
        "s_cmp_eq_u32 %[ph], 3\n\t"
        "s_cselect_b32 %[ph], 0, %[ph]\n\t"
        "s_cmp_eq_u32 %[ph], 2\n\t"
        "s_cselect_b32 %[delta], %[cm2], %[cp1]\n\t"
        "s_cmp_eq_u32 %[ph], 1\n\t"
        "s_cselect_b32 m0, %[cm2], %[cp1]\n\t"
        "s_add_u32 %[m0n], %[m0n], m0\n\t"
        "s_add_u32 %[soff], %[soff], 8192\n\t"
        "s_cmp_gt_u32 %[cnt], 4\n\t"
        "s_cselect_b32 %[soff], %[oob], %[soff]\n\t"
        "s_add_u32 %[cnt], %[cnt], 1\n\t"
        "s_cmp_lt_u32 %[cnt], 8\n\t"
        "s_cbranch_scc1 1b\n\t"
        MF(R_OB, R_V1, R_P1, R_OB)
        "s_waitcnt vmcnt(0) lgkmcnt(0)\n\ts_nop 15\n\t"
        : "+{v[16:31]}"(oa), "+{v[32:47]}"(ob), [aK] "+&v"(aK), [aV0] "+&v"(aV0), [aV1] "+&v"(aV1), [cnt] "+&s"(cnt), [ph] "+&s"(ph),
          [delta] "+&s"(delta), [m0n] "+&s"(m0n), [soff] "+&s"(soff)
        : "{v[80:95]}"(cneg), "{v[96:99]}"(qa), "{v[100:103]}"(qbf), [voff] "v"(voff), [skv] "s"(skv),
          [cp1] "s"(AT_SLOT), [cm2] "s"(-2 * AT_SLOT), [oob] "s"(OOB)
        : "memory", "scc", "v48", "v49", "v50", "v51", "v52", "v53", "v54", "v55", "v56", "v57", "v58", "v59", "v60", "v61", "v62", "v63", "v64",
          "v65", "v66", "v67", "v68", "v69", "v70", "v71", "v72", "v73", "v74", "v75", "v76", "v77", "v78", "v79", "v104", "v105", "v106",
          "v107", "v108", "v109", "v110", "v111", "v112", "v113", "v114", "v115", "v116", "v117", "v118", "v119", "v120", "v121", "v122", "v123");
  } else {
    ma = -INFINITY; mb = -INFINITY;
    const s16x8 qf[2] = {__builtin_bit_cast(s16x8, qa), __builtin_bit_cast(s16x8, qbf)};
    int slot = 0;
#pragma unroll 1
    for (int c = 0; c < 8; ++c) {
#pragma unroll 1
      for (int jt = 0; jt < 4; ++jt) {
        const unsigned so = (unsigned)(slot * AT_SLOT + jt * AT_TILE);
        const s16x8 kf = lds_frag(aK + so), v0 = lds_frag(aV0 + so), v1 = lds_frag(aV1 + so);
#pragma unroll
        for (int qi = 0; qi < 2; ++qi) {
          f32x16& o = qi ? ob : oa;
          float& m = qi ? mb : ma;
          f32x16 s = MFMA(kf, qf[qi], f32x16{});
          float tm = s[0];
#pragma unroll
          for (int i = 1; i < 16; ++i) tm = fmaxf(tm, s[i]);
          const float mn = fmaxf(m, red_x32<true>(tm));
          const float f = __builtin_amdgcn_exp2f(m - mn);
          m = mn;
#pragma unroll
          for (int i = 0; i < 16; ++i) { o[i] *= f; s[i] = __builtin_amdgcn_exp2f(s[i] - mn); }
          const u32x4 p0 = {pk_bf16(s[0], s[1]), pk_bf16(s[2], s[3]), pk_bf16(s[4], s[5]), pk_bf16(s[6], s[7])};
          const u32x4 p1 = {pk_bf16(s[8], s[9]), pk_bf16(s[10], s[11]), pk_bf16(s[12], s[13]), pk_bf16(s[14], s[15])};
          o = MFMA(v0, __builtin_bit_cast(s16x8, p0), o);
          o = MFMA(v1, __builtin_bit_cast(s16x8, p1), o);
        }
      }
      wait_bar<0>();
      if (c < 6) issue(c + 2, slot == 0 ? 2 : slot - 1);
      slot = slot == 2 ? 0 : slot + 1;
    }
  }
  STAMP(7);
  {
    const unsigned ex = lds0 + AT_EX + w * 2560;
    const f32x16& snd = kh ? oa : ob;
    lds_st<f32x4>(ex + lane * 16, f32x4{snd[0], snd[1], snd[2], snd[3]});
    lds_st<f32x4>(ex + 1024 + lane * 16, f32x4{snd[4], snd[5], snd[6], snd[7]});
    lds_st<f32x2>(ex + 2048 + lane * 8, f32x2{snd[8], kh ? ma : mb});
  }
  lds_bar();
  {
    const unsigned ex = lds0 + AT_EX + (w ^ 4) * 2560;
    const f32x16& own = kh ? ob : oa;
    const float mo = kh ? mb : ma;
    const f32x4 t0 = lds_ld<f32x4>(ex + lane * 16), t1 = lds_ld<f32x4>(ex + 1024 + lane * 16);
    const f32x2 t2 = lds_ld<f32x2>(ex + 2048 + lane * 8);
    const float mm = fmaxf(mo, t2[1]);
    const float f0 = __builtin_amdgcn_exp2f(mo - mm), f1 = __builtin_amdgcn_exp2f(t2[1] - mm);
    const float inv = __builtin_amdgcn_rcpf(own[8] * f0 + t2[0] * f1);
    const float g0 = f0 * inv, g1 = f1 * inv;
    const u32x4 pk = {pk_bf16(own[0] * g0 + t0[0] * g1, own[1] * g0 + t0[1] * g1), pk_bf16(own[2] * g0 + t0[2] * g1, own[3] * g0 + t0[3] * g1),
                      pk_bf16(own[4] * g0 + t1[0] * g1, own[5] * g0 + t1[1] * g1), pk_bf16(own[6] * g0 + t1[2] * g1, own[7] * g0 + t1[3] * g1)};
    st16c(sws, WS_O + (unsigned)(((qt + kh) * 32 + head) * 1024) + voff, pk);
  }
  publish(ws, WS_FO + (qb * 32 + head) * 8, tag);
}

DI float geglu(float g, float u) {
  const float c1 = -2.0f * 0.7978845608028654f * 1.4426950408889634f, c2 = c1 * 0.044715f;
  const float e = __builtin_amdgcn_exp2f(g * __builtin_fmaf(c2 * g, g, c1));
  return g * u * __builtin_amdgcn_rcpf(1.0f + e);
}

DI void ln_phase(const char* x, const char* wqkv, char* ws, unsigned lds0, int j, int w, unsigned& epoch) {
  const int lane = TID & 63;
  u32x4 xv;
  asm volatile("global_load_dwordx4 %0, %1, %2" : "=v"(xv) : "v"(lane * 16), "s"(x + (8 * j + w) * 1024) : "memory");
  {
    unsigned lo, hi, len;
    asm volatile("s_getpc_b64 vcc\n\t.Lpf_here:\n\ts_sub_u32 %0, vcc_lo, .Lpf_here-" KNAME "\n\ts_subb_u32 %1, vcc_hi, 0\n\ts_mov_b32 %2, fused_block_end-" KNAME
                 : "=s"(lo), "=s"(hi), "=s"(len)::"vcc", "scc");
    dma16(lds0 + 16384 + w * 1024, lane * 16, make_srd((const void*)(((u64)hi << 32) | lo), len), (unsigned)(((j >> 3) * 8 + w) * 1024));
  }
  epoch = get_epoch(ws);
  asm volatile("s_waitcnt vmcnt(%1)" : "+v"(xv) : "i"((PREFETCH ? 5 : 0) + (CODEPF ? 1 : 0)) : "memory");
  float f[8];
#pragma unroll
  for (int i = 0; i < 4; ++i) { f[2 * i] = bf_lo(xv[i]); f[2 * i + 1] = bf_hi(xv[i]); }
  const float mean = red_x32<false>(red32<false>(((f[0] + f[1]) + (f[2] + f[3])) + ((f[4] + f[5]) + (f[6] + f[7])))) * (1.0f / DM);
  float q = 0.f;
#pragma unroll
  for (int i = 0; i < 8; ++i) q = __builtin_fmaf(f[i] - mean, f[i] - mean, q);
  const float var = red_x32<false>(red32<false>(q)) * (1.0f / DM);
  const float m = rbf(mean), dd = rbf(__builtin_amdgcn_sqrtf(rbf(rbf(var) + 1e-5f))), a = __builtin_amdgcn_rcpf(dd);
  if (lane == 0) flag_store(ws, WS_GS + (8 * j + w) * 8, ((u64)~epoch << 32) | __float_as_uint(m) | (__float_as_uint(dd) >> 16));
  u32x4 pk;
#pragma unroll
  for (int i = 0; i < 4; ++i) { const unsigned d = pk_bf16(f[2 * i] - m, f[2 * i + 1] - m); pk[i] = pk_bf16(bf_lo(d) * a, bf_hi(d) * a); }
  lds_st<u32x4>(lds0 + w * 1040 + lane * 16, pk);
  lds_bar();
  const int row = 8 * j + (lane & 7), ch = 8 * w + (lane >> 3);
  pk = lds_ld<u32x4>(lds0 + (lane & 7) * 1040 + ch * 16);
  st16c(make_srd(ws, WS_END), WS_XN + (unsigned)((((row >> 5) * 32 + (ch >> 1)) * 64 + (ch & 1) * 32 + (row & 31)) * 16), pk);
  publish(ws, WS_FL + j * 8, make_tag(epoch));
}

DI void up_phase(const char* wg, const char* wu, char* ws, unsigned lds0, int j, int w, u64 tag STAMP_ARG) {
  const int lane = TID & 63, hh = lane >> 5, tp = w >> 2, ft = w & 3;
  const int xcd = j & 7, i = j >> 3, tb = 4 * (xcd >> 1) + (i & 3), fb = 7 * (xcd & 1) + (i >> 2);
  if (w == 0) poll(ws, lane < 16 ? WS_FL + (tb * 16 + lane) * 8 : WS_FS + j * 8, tag);
  lds_bar();
  STAMP(2);
  const srd_t sx = make_srd(ws + WS_XN, 2u << 20), sg = make_srd(wg, DM * DF * 2), su = make_srd(wu, DM * DF * 2);
  const unsigned voX = lane * 16, soX = (unsigned)(((tb * 4 + (w >> 1)) * 32 + (w & 1)) * 1024);
  const unsigned voW = (unsigned)((4 * w + (lane >> 4)) * (DF * 2) + fb * 256 + (((lane & 15) ^ (((lane >> 4) & 3) << 2)) * 16));
  const int rot = ROTU ? 4 * (i & 3) + (i >> 2) : 0;
  auto issue = [&](int st, unsigned slot) {
    const unsigned sl = lds0 + slot + (unsigned)w * 1024u;
    const unsigned k = (unsigned)(st + rot) & 15u, sw_ = st < 16 ? k * (32 * DF * 2) : OOB;
    dma16c(sl, voX, sx, st < 16 ? soX + k * 2048 : OOB);
    dma16(sl + 8192, voW, sg, sw_);
    dma16(sl + 16384, voW, su, sw_);
  };
  issue(0, 0); issue(1, 24576); issue(2, 49152);
  const int q = (lane & 15) >> 2;
  const unsigned aX = lds0 + tp * 4096 + lane * 16;
  const unsigned aG = lds0 + 8192 + (unsigned)((8 * hh + q) * 256 + ((ft ^ q) * 64) + ((lane >> 4) & 1) * 32 + (lane & 3) * 8);
  f32x16 ga = {}, gb = {}, ua = {}, ub = {};
  s16x8 xa0, xb0, fg0, fu0, xa1, xb1, fg1, fu1;
#define URD(XA, XB, FG, FU, SL, S) do { XA = lds_frag(aX + (SL) + (S) * 1024); XB = lds_frag(aX + (SL) + 2048 + (S) * 1024); \
    FG = lds_tr_frag(aG + (SL) + (S) * 4096, aG + (SL) + (S) * 4096 + 1024); FU = lds_tr_frag(aG + (SL) + 8192 + (S) * 4096, aG + (SL) + 8192 + (S) * 4096 + 1024); SBAR(); } while (0)
#define UMM(XA, XB, FG, FU) do { ga = MFMA(FG, XA, ga); gb = MFMA(FG, XB, gb); ua = MFMA(FU, XA, ua); ub = MFMA(FU, XB, ub); SBAR(); } while (0)
#define USTAGE(ST, SL, SN) do { URD(xa1, xb1, fg1, fu1, SL, 1); UMM(xa0, xb0, fg0, fu0); wait_bar<3>(); \
    URD(xa0, xb0, fg0, fu0, SN, 0); UMM(xa1, xb1, fg1, fu1); issue((ST) + 3, SL); } while (0)
  wait_bar<6>();
  URD(xa0, xb0, fg0, fu0, 0, 0);
#pragma unroll 1
  for (int st = 0; st < 15; st += 3) { USTAGE(st, 0, 24576); USTAGE(st + 1, 24576, 49152); USTAGE(st + 2, 49152, 0); }
  URD(xa1, xb1, fg1, fu1, 0, 1); UMM(xa0, xb0, fg0, fu0); UMM(xa1, xb1, fg1, fu1);
#undef USTAGE
#undef URD
#undef UMM
  STAMP(3);
  __builtin_amdgcn_s_setprio(3);
  const srd_t sws = make_srd(ws, WS_END);
  const unsigned ho = WS_H + (unsigned)(((tb * 4 + tp * 2) * 112 + fb * 8 + ft * 2) * 1024) + lane * 16;
#pragma unroll
  for (int t = 0; t < 2; ++t) {
    const f32x16& g = t ? gb : ga;
    const f32x16& u = t ? ub : ua;
#pragma unroll
    for (int e = 0; e < 2; ++e) {
      u32x4 pk;
#pragma unroll
      for (int k = 0; k < 4; ++k) pk[k] = pk_bf16(geglu(g[8 * e + 2 * k], u[8 * e + 2 * k]), geglu(g[8 * e + 2 * k + 1], u[8 * e + 2 * k + 1]));
      st16c(sws, ho + t * 112 * 1024 + e * 1024, pk);
    }
  }
  __builtin_amdgcn_s_setprio(0);
  publish(ws, WS_FH + (tb * 16 + fb) * 8, tag);
}

DI void out_phase(const char* x, const char* wd, const char* wo, char* ws, char* out, unsigned lds0, int j, int w, u64 tag STAMP_ARG) {
  const int lane = TID & 63, r = lane & 31, hh = lane >> 5;
  const int xcd = j & 7, i = j >> 3, mb = 4 * xcd + (i & 3), nb = i >> 2;
  const int tl = w & 1, nt = (w >> 1) & 1, kh = w >> 2;
  const unsigned voX = lane * 16, soH = (unsigned)(((2 * mb + (w >> 2)) * 112 + (w & 3)) * 1024);
  const unsigned voW = (unsigned)((8 * w + (lane >> 3)) * 1024 + nb * 128 + (((lane & 7) ^ (((lane >> 4) & 1) << 2)) * 16));
  {
    const srd_t sw = make_srd(wo, DM * DM * 2);
#pragma unroll
    for (int st = 0; st < 8; ++st) dma16(lds0 + st * 8192 + w * 1024, voW, sw, st * 65536);
  }
  const int ocol = 64 * nb + 32 * nt + 16 * kh + 4 * hh, orow = 64 * mb + 32 * tl + r;
  const u32x2 xr0 = *(const u32x2*)(x + orow * 1024 + ocol * 2), xr1 = *(const u32x2*)(x + orow * 1024 + ocol * 2 + 16);
  if (w == 0) poll(ws, WS_FH + ((mb >> 1) * 16 + (lane < 14 ? lane : 0)) * 8, tag);
  wait_bar<0>();
  const int q = (lane & 15) >> 2;
  const unsigned aX = lds0 + tl * 4096 + kh * 2048 + lane * 16;
  const unsigned aW = lds0 + (unsigned)((32 * kh + 4 * hh + q) * 128 + ((nt ^ (q >> 1)) * 64) + ((lane >> 4) & 1) * 32 + (lane & 3) * 8);
  s16x8 wf[16];
#pragma unroll
  for (int k = 0; k < 16; ++k) wf[k] = lds_tr_frag(aW + (k >> 1) * 8192 + (k & 1) * 2048, aW + (k >> 1) * 8192 + (k & 1) * 2048 + 1024);
  lds_bar();
  STAMP(5);
  const srd_t sh = make_srd(ws + WS_H, 64u * 112u * 1024u), sd = make_srd(wd, DF * DM * 2);
  const int rot = ROTD ? (7 * (i & 3) + 3 * (i >> 2)) % 28 : 0;
  auto issue = [&](int st, unsigned slot) {
    const unsigned sl = lds0 + slot + (unsigned)w * 1024u;
    const unsigned k = (unsigned)(st + rot >= 28 ? st + rot - 28 : st + rot);
    dma16c(sl, voX, sh, st < 28 ? soH + k * 4096 : OOB);
    dma16(sl + 8192, voW, sd, st < 28 ? k * 65536 : OOB);
  };
#pragma unroll
  for (int st = 0; st < 4; ++st) issue(st, st * 16384);
  f32x16 acc = {};
  {
    s16x8 x0, w0, x1, w1;
#define DRD(X, W, SL, E) do { X = lds_frag(aX + (SL) + (E) * 1024); W = lds_tr_frag(aW + 8192 + (SL) + (E) * 2048, aW + 8192 + (SL) + (E) * 2048 + 1024); SBAR(); } while (0)
#define DSTAGE(ST, SL, SN) do { DRD(x1, w1, SL, 1); acc = MFMA(w0, x0, acc); SBAR(); wait_bar<4>(); \
    DRD(x0, w0, SN, 0); acc = MFMA(w1, x1, acc); SBAR(); issue((ST) + 4, SL); } while (0)
    wait_bar<6>();
    DRD(x0, w0, 0, 0);
#pragma unroll 1
    for (int st = 0; st < 28; st += 4) { DSTAGE(st, 0, 16384); DSTAGE(st + 1, 16384, 32768); DSTAGE(st + 2, 32768, 49152); DSTAGE(st + 3, 49152, 0); }
#undef DSTAGE
#undef DRD
  }
  STAMP(7);
  if (w == 0) poll(ws, WS_FO + ((mb >> 2) * 32 + r) * 8, tag);
  wait_bar<0>();
  STAMP(8);
  {
    const srd_t so = make_srd(ws + WS_O, 2u << 20);
    const unsigned soO = (unsigned)(((2 * mb + (w >> 2)) * 32 + (w & 3)) * 1024);
#pragma unroll
    for (int st = 0; st < 8; ++st) dma16c(lds0 + st * 8192 + w * 1024, voX, so, soO + st * 4096);
  }
#pragma unroll
  for (int g = 0; g < 4; ++g) {
    if (g == 0) wait_bar<6>(); else if (g == 1) wait_bar<4>(); else if (g == 2) wait_bar<2>(); else wait_bar<0>();
    s16x8 xf[4];
#pragma unroll
    for (int k = 0; k < 4; ++k) xf[k] = lds_frag(aX + (2 * g + (k >> 1)) * 8192 + (k & 1) * 1024);
#pragma unroll
    for (int k = 0; k < 4; ++k) acc = MFMA(wf[4 * g + k], xf[k], acc);
  }
  STAMP(9);
  {
    const unsigned ex = lds0 + 65536 + w * 2048 + lane * 16;
    lds_st<f32x4>(ex, kh ? f32x4{acc[0], acc[1], acc[2], acc[3]} : f32x4{acc[8], acc[9], acc[10], acc[11]});
    lds_st<f32x4>(ex + 1024, kh ? f32x4{acc[4], acc[5], acc[6], acc[7]} : f32x4{acc[12], acc[13], acc[14], acc[15]});
  }
  lds_bar();
  {
    const unsigned ex = lds0 + 65536 + (w ^ 4) * 2048 + lane * 16;
    const f32x4 t0 = lds_ld<f32x4>(ex), t1 = lds_ld<f32x4>(ex + 1024);
    float v[8];
#pragma unroll
    for (int k = 0; k < 4; ++k) { v[k] = (kh ? acc[8 + k] : acc[k]) + t0[k]; v[4 + k] = (kh ? acc[12 + k] : acc[4 + k]) + t1[k]; }
    const u32x2 o0 = {pk_bf16(v[0] + bf_lo(xr0[0]), v[1] + bf_hi(xr0[0])), pk_bf16(v[2] + bf_lo(xr0[1]), v[3] + bf_hi(xr0[1]))};
    const u32x2 o1 = {pk_bf16(v[4] + bf_lo(xr1[0]), v[5] + bf_hi(xr1[0])), pk_bf16(v[6] + bf_lo(xr1[1]), v[7] + bf_hi(xr1[1]))};
    *(u32x2*)(out + orow * 1024 + ocol * 2) = o0;
    *(u32x2*)(out + orow * 1024 + ocol * 2 + 16) = o1;
  }
}

extern "C" __global__ __launch_bounds__(512, 4) void fused_block(const char* __restrict__ x, const char* __restrict__ wqkv, const char* __restrict__ wo,
                                                      const char* __restrict__ wg, const char* __restrict__ wu, const char* __restrict__ wd,
                                                      char* ws, char* __restrict__ out) {
  extern __shared__ __attribute__((aligned(16))) char lds[];
  const unsigned lds0 = (unsigned)(uintptr_t)(LDSP char*)lds;
  STAMP_DECL; STAMP(0);
  int w = __builtin_amdgcn_readfirstlane(TID >> 6);
  asm volatile("s_nop 4" : "+s"(w));
  unsigned epoch;
  asm volatile("s_mov_b32 %0, 0" : "=s"(epoch));
  const int b = BID & 255, rb = 4 * ((b & 7) >> 1) + ((b >> 3) & 3), cb = 8 * (b & 1) + (b >> 5);
  if (__builtin_expect(BID >= 256 && w >= 4, 1)) {
    __builtin_amdgcn_s_setprio(2);
    qkv_loader<0>(x, wqkv, ws, lds0, b, rb, cb, w, epoch STAMP_PASS);
  }
  asm volatile("" ::: "memory");
  if (__builtin_expect(BID < 256, 1)) ln_phase(x, wqkv, ws, lds0, b, w, epoch);
  asm volatile("" ::: "memory");
  if (__builtin_expect(BID >= 256 && w >= 4, 1)) qkv_loader<1>(x, wqkv, ws, lds0, b, rb, cb, w, epoch STAMP_PASS);
  if (BID >= 256) {
    if (w < 4) {
      __builtin_amdgcn_s_setprio(2);
      if (cb < 10) qkv_wave<0>(ws, lds0, rb, cb, w, epoch STAMP_PASS);
      else if (cb == 10) qkv_wave<1>(ws, lds0, rb, cb, w, epoch STAMP_PASS);
      else qkv_wave<3>(ws, lds0, rb, cb, w, epoch STAMP_PASS);
    }
    const u64 tag = make_tag(epoch);
    publish(ws, WS_FQ + (cb * 16 + rb) * 8, tag);
    if (HINT_AT == 1 && TID == 0) flag_store(ws, WS_FS + b * 8, tag);
    STAMP(4);
    attn_phase(ws, lds0, b, w, tag STAMP_PASS);
  } else {
    const int j = BID;
    const u64 tag = make_tag(epoch);
    STAMP(1);
    if (j < 224) {
      up_phase(wg, wu, ws, lds0, j, w, tag STAMP_PASS);
    } else if (j == 255 && w == 1) {
      const int lane = TID & 63;
#pragma unroll 1
      for (int k = 0; k < 8; ++k) poll(ws, WS_FQ + (k * 64 + lane) * 8, tag);
      if (lane == 0) __hip_atomic_store((gu32*)(ws + WS_EP), epoch, __ATOMIC_RELAXED, __HIP_MEMORY_SCOPE_AGENT);
    }
    STAMP(4);
    out_phase(x, wd, wo, ws, out, lds0, j, w, tag STAMP_PASS);
  }
  STAMP_FLUSH(ws);
}

extern "C" __device__ __attribute__((noinline, used, visibility("hidden"))) void fused_block_end() {}

extern "C" void kernel_launch(void* const* d_in, const int* in_sizes, int n_in, void* d_out, int out_size, void* d_ws, size_t ws_size,
                              hipStream_t stream) {
  (void)hipFuncSetAttribute((const void*)fused_block, hipFuncAttributeMaxDynamicSharedMemorySize, LDS_BYTES);
  fused_block<<<512, 512, LDS_BYTES, stream>>>((const char*)d_in[0], (const char*)d_in[1], (const char*)d_in[2], (const char*)d_in[4],
                                               (const char*)d_in[5], (const char*)d_in[6], (char*)d_ws, (char*)d_out);
}

#if defined(__HIP_DEVICE_COMPILE__)
#pragma clang attribute pop
#endif
```

```cpp
#if defined(__HIP_DEVICE_COMPILE__)
#pragma clang attribute push(__attribute__((target("no-packed-fp32-ops"))), apply_to = function)
#endif
#include <hip/hip_runtime.h>
#include <stdint.h>

typedef float f32x16 __attribute__((ext_vector_type(16)));
typedef float f32x4 __attribute__((ext_vector_type(4)));
typedef float f32x2 __attribute__((ext_vector_type(2)));
typedef short s16x8 __attribute__((ext_vector_type(8)));
typedef short s16x4 __attribute__((ext_vector_type(4)));
typedef __bf16 bf16x2 __attribute__((ext_vector_type(2)));
typedef unsigned u32x4 __attribute__((ext_vector_type(4)));
typedef unsigned u32x2 __attribute__((ext_vector_type(2)));
typedef unsigned long long u64;
typedef __attribute__((address_space(1))) u64 gu64;
typedef __attribute__((address_space(1))) unsigned gu32;
typedef __amdgpu_buffer_rsrc_t srd_t;
#define LDSP __attribute__((address_space(3)))
#define DI __device__ __forceinline__
#define MFMA(a, b, c) __builtin_amdgcn_mfma_f32_32x32x16_bf16((a), (b), (c), 0, 0, 0)
#define SBAR() __builtin_amdgcn_sched_barrier(0)
#define TID ((int)__builtin_amdgcn_workitem_id_x())
#define BID ((int)__builtin_amdgcn_workgroup_id_x())
#define POLL_SLEEP 4
#define TLBPF 0
#define MAP2 1
#define LPRIO 2
#define UPRIO 0
#define UPRIO_H 16
#define HWAIT 16
#define WPF 2
#define CODEPF 1
#define KNAME "fused_block"
#define ROTQ 1
#define ROTU 2
#define ROTD 1
#define PREFETCH 0
#define HINT_AT 0
#define HINT_ST 3

constexpr int SEQ = 2048, DM = 512, DF = 1792;
constexpr unsigned WS_XN = 0;
constexpr unsigned WS_Q = WS_XN + (2u << 20);
constexpr unsigned WS_KV = WS_Q + (2u << 20);
constexpr unsigned WS_O = WS_KV + (4u << 20);
constexpr unsigned WS_H = WS_O + (2u << 20);
constexpr unsigned WS_KN = WS_H + 64u * 112u * 1024u;
constexpr unsigned WS_GS = WS_KN + 16384;
constexpr unsigned WS_FQ = WS_GS + 16384;
constexpr unsigned WS_FL = WS_FQ + 2048;
constexpr unsigned WS_FH = WS_FL + 2048;
constexpr unsigned WS_FO = WS_FH + 2048;
constexpr unsigned WS_FS = WS_FO + 2048;
constexpr unsigned WS_END = WS_FS + 2048;
#define NREG 8
constexpr unsigned WS_REG = 18u << 20;
constexpr unsigned WS_EP = NREG * WS_REG;
static_assert(WS_END <= WS_REG && WS_EP + 256 <= (200u << 20), "workspace map");
constexpr float QSCALE = 0.25f * 1.4426950408889634f;
constexpr float BOUND_LIMIT = 48.0f;
constexpr unsigned OOB = 0x40000000u;
constexpr int LDS_BYTES = 81920;

DI bool all_lanes(bool c) { return __builtin_amdgcn_ballot_w64(!c) == 0; }
DI srd_t make_srd(const void* p, unsigned bytes) { return __builtin_amdgcn_make_buffer_rsrc((void*)p, (short)0, (int)bytes, 0x00020000); }
DI unsigned pk_bf16(float lo, float hi) { f32x2 v = {lo, hi}; bf16x2 b = __builtin_convertvector(v, bf16x2); return __builtin_bit_cast(unsigned, b); }
DI float rbf(float v) { return __uint_as_float(pk_bf16(v, v) << 16); }
DI float bf_lo(unsigned u) { return __uint_as_float(u << 16); }
DI float bf_hi(unsigned u) { return __uint_as_float(u & 0xffff0000u); }
template <bool MAX> DI float comb(float a, float b) { return MAX ? __uint_as_float(__builtin_elementwise_max(__float_as_uint(a), __float_as_uint(b))) : a + b; }
template <bool MAX, int CTRL> DI float red_dpp(float v) { return comb<MAX>(v, __int_as_float(__builtin_amdgcn_update_dpp(0, __float_as_int(v), CTRL, 0xf, 0xf, true))); }
template <bool MAX> DI float red_x16(float v) { auto r = __builtin_amdgcn_permlane16_swap(__float_as_uint(v), __float_as_uint(v), false, false); return comb<MAX>(__uint_as_float(r[0]), __uint_as_float(r[1])); }
template <bool MAX> DI float red_x32(float v) { auto r = __builtin_amdgcn_permlane32_swap(__float_as_uint(v), __float_as_uint(v), false, false); return comb<MAX>(__uint_as_float(r[0]), __uint_as_float(r[1])); }
template <bool MAX> DI float red32(float v) {
  v = red_dpp<MAX, 0xB1>(v); v = red_dpp<MAX, 0x4E>(v); v = red_dpp<MAX, 0x141>(v); v = red_dpp<MAX, 0x140>(v); return red_x16<MAX>(v);
}
DI void dma16(unsigned lds, unsigned voff, srd_t srd, unsigned soff) {
  asm volatile("s_mov_b32 m0, %0\n\ts_nop 0\n\tbuffer_load_dwordx4 %1, %2, %3 offen lds" ::"s"(lds), "v"(voff), "s"(srd), "s"(soff) : "memory");
}
DI void dma16c(unsigned lds, unsigned voff, srd_t srd, unsigned soff) {
  asm volatile("s_mov_b32 m0, %0\n\ts_nop 0\n\tbuffer_load_dwordx4 %1, %2, %3 offen sc1 lds" ::"s"(lds), "v"(voff), "s"(srd), "s"(soff) : "memory");
}
DI void touch(unsigned lds, unsigned voff, srd_t srd, unsigned soff) {
  asm volatile("s_mov_b32 m0, %0\n\ts_nop 0\n\tbuffer_load_dword %1, %2, %3 offen lds" ::"s"(lds), "v"(voff), "s"(srd), "s"(soff) : "memory");
}
template <int N> DI void wait_bar() { asm volatile("s_waitcnt vmcnt(%0) lgkmcnt(0)\n\ts_barrier" ::"i"(N) : "memory"); }
DI void lds_bar() { asm volatile("s_waitcnt lgkmcnt(0)\n\ts_barrier" ::: "memory"); }
DI s16x8 lds_frag(unsigned a) { return *(LDSP const s16x8*)(uintptr_t)a; }
DI s16x8 lds_tr_frag(unsigned a0, unsigned a1) {
  s16x4 a = __builtin_amdgcn_ds_read_tr16_b64_v4i16((LDSP s16x4*)(uintptr_t)a0), b = __builtin_amdgcn_ds_read_tr16_b64_v4i16((LDSP s16x4*)(uintptr_t)a1);
  return __builtin_shufflevector(a, b, 0, 1, 2, 3, 4, 5, 6, 7);
}
template <class T> DI T lds_ld(unsigned a) { return *(LDSP const T*)(uintptr_t)a; }
template <class T> DI void lds_st(unsigned a, T v) { *(LDSP T*)(uintptr_t)a = v; }
DI void st16c(srd_t srd, unsigned voff, u32x4 v) { __builtin_amdgcn_raw_buffer_store_b128(v, srd, voff, 0, 16); }
DI u64 make_tag(unsigned e) { return ((u64)(e ^ 0x5bd1e995u) << 32) | e; }
DI unsigned get_epoch(char*& ws) {
  unsigned e;
  asm volatile("s_load_dword %0, %1, %2\n\ts_waitcnt lgkmcnt(0)" : "=s"(e) : "s"(ws), "s"(WS_EP) : "memory");
  e += 1u;
  ws += (size_t)((e % NREG) * WS_REG);
  return e;
}
DI void flag_store(char* ws, unsigned off, u64 tag) { __hip_atomic_store((gu64*)(ws + off), tag, __ATOMIC_RELAXED, __HIP_MEMORY_SCOPE_AGENT); }
DI void publish(char* ws, unsigned off, u64 tag) { wait_bar<0>(); if (TID == 0) flag_store(ws, off, tag); }
DI void poll(char* ws, unsigned off, u64 tag) {
#pragma unroll 1
  for (int it = 0; it < (1 << 20); ++it) {
    const u64 v = __hip_atomic_load((gu64*)(ws + off), __ATOMIC_RELAXED, __HIP_MEMORY_SCOPE_AGENT);
    if (all_lanes(v == tag)) break;
    __builtin_amdgcn_s_sleep(POLL_SLEEP);
  }
}

#define STAMP_DECL do {} while (0)
#define STAMP(i) do {} while (0)
#define STAMP_FLUSH(ws) do {} while (0)
#define STAMP_ARG
#define STAMP_PASS

constexpr unsigned Q_W = 49152, Q_STAT = Q_W + 5 * 6144, Q_CS = 32768;

template <int PART>
DI void qkv_loader(const char* x, const char* wqkv, char*& ws, unsigned lds0, int b, int rb, int cb, int w, unsigned& epoch STAMP_ARG) {
  const int lane = TID & 63, L = w - 4;
  const srd_t sx = make_srd(x, SEQ * DM * 2), sw = make_srd(wqkv, DM * 3 * DM * 2);
  const unsigned voX0 = (unsigned)((rb * 128 + 32 * L + (lane >> 3)) * 1024 + (((lane & 7) ^ (lane >> 4)) * 16));
  const unsigned voX1 = (unsigned)((rb * 128 + 32 * L + 8 + (lane >> 3)) * 1024 + (((lane & 7) ^ (4 + (lane >> 4))) * 16)) - 1024u;
  const unsigned voW0 = (unsigned)((8 * L + lane / 12) * 3072 + cb * 192 + (lane % 12) * 16), voW1 = voW0 + 4 * 3072 - 768;
  const unsigned rot = ROTQ ? (unsigned)(cb + 2 * rb) : 0u;
  auto group = [&](unsigned hslot, unsigned hs, unsigned xslot, unsigned st, int part) {
    asm volatile("s_mov_b32 m0, %0\n\ts_mov_b32 exec_hi, 0xffff\n\tbuffer_load_dwordx4 %1, %3, %4 offen lds\n\tbuffer_load_dwordx4 %2, %3, %4 offen offset:768 lds\n\t"
                 "s_mov_b32 exec_hi, -1"
                 ::"s"(lds0 + Q_W + hslot + L * 1536), "v"(voW0), "v"(voW1), "s"(sw), "s"(((hs + 2 * rot) & 15) * 98304) : "memory");
    asm volatile("s_mov_b32 m0, %0\n\ts_nop 0\n\tbuffer_load_dwordx4 %1, %3, %4 offen lds\n\tbuffer_load_dwordx4 %2, %3, %4 offen offset:1024 lds"
                 ::"s"(lds0 + xslot + L * 4096 + part * 2048), "v"(voX0), "v"(voX1), "s"(sx), "s"(((st + rot) & 7) * 128 + part * 16384) : "memory");
  };
  if (PART == 0) {
    STAMP(8);
    group(0, 0, 0, 0, 0); group(6144, 1, 0, 0, 1); group(2 * 6144, 2, 16384, 1, 0); group(3 * 6144, 3, 16384, 1, 1);
    STAMP(10);
    return;
  }
  epoch = get_epoch(ws);
  STAMP(1);
#pragma unroll
  for (int st = 0; st < 6; ++st) {
    wait_bar<8>();
    group(((2 * st + 4) % 5) * 6144, 2 * st + 4, ((st + 2) % 3) * 16384, st + 2, 0);
    wait_bar<12>();
    group(((2 * st + 5) % 5) * 6144, 2 * st + 5, ((st + 2) % 3) * 16384, st + 2, 1);
    if (HINT_AT == 0 && st == HINT_ST && TID == 448) flag_store(ws, WS_FS + b * 8, make_tag(epoch));
  }
  wait_bar<8>();
  const srd_t sws = make_srd(ws, WS_END);
  const unsigned go = WS_GS + rb * 1024 + lane * 16;
  u32x4 gs;
  asm volatile("buffer_load_dwordx4 %0, %1, %2, 0 offen sc1" : "=v"(gs) : "v"(go), "s"(sws) : "memory");
  wait_bar<9>();
  asm volatile("s_waitcnt vmcnt(0)\n\ts_barrier" : "+v"(gs)::"memory");
  if (L == 0) {
#pragma unroll 1
    for (int it = 0; it < (1 << 16) && !all_lanes(gs[1] == ~epoch && gs[3] == ~epoch); ++it) gs = __builtin_amdgcn_raw_buffer_load_b128(sws, go, 0, 16);
    lds_st<f32x2>(lds0 + Q_STAT + lane * 8, f32x2{bf_hi(gs[0]), bf_hi(gs[2])});
    lds_st<f32x2>(lds0 + Q_STAT + 512 + lane * 8, f32x2{__builtin_amdgcn_rcpf(bf_lo(gs[0])), __builtin_amdgcn_rcpf(bf_lo(gs[2]))});
  }
  lds_bar();
  STAMP(3);
}

template <int NV>
DI void qkv_wave(char*& ws, unsigned lds0, int rb, int cb, int rt, unsigned& epoch STAMP_ARG) {
  const int lane = TID & 63, r = lane & 31, hh = lane >> 5;
  epoch = get_epoch(ws);
  const int swz = (r >> 1) & 7, sw = rt & 1;
  unsigned aX[4];
#pragma unroll
  for (int s = 0; s < 4; ++s) aX[s] = lds0 + (unsigned)((32 * rt + r) * 128 + (((2 * (s ^ sw) + hh) ^ swz) * 16));
  const unsigned aWb = lds0 + Q_W + (unsigned)((8 * hh + ((lane & 15) >> 2)) * 192 + ((lane >> 4) & 1) * 32 + (lane & 3) * 8);
  const unsigned aW[2] = {aWb + sw * 3072, aWb + (sw ^ 1) * 3072};
  f32x16 acc[3] = {};
  f32x4 csum[3] = {};
  s16x4 ones = {0x3f80, 0x3f80, 0x3f80, 0x3f80};
  asm volatile("" : "+v"(ones));
  s16x8 xf[3];
  s16x4 wl[3][3], wh[3][3];
#define TRD(A) __builtin_amdgcn_ds_read_tr16_b64_v4i16((LDSP s16x4*)(uintptr_t)(A))
#define QRX(F, S) xf[F] = lds_frag(aX[S] + xs)
#define QRL(F, J) wl[F][J] = TRD(aW[(F) != 0] + hr + 64 * (J))
#define QRH(F, J) wh[F][J] = TRD(aW[(F) != 0] + hr + 768 + 64 * (J))
#define QM(F, J) do { const s16x8 w_ = __builtin_shufflevector(wl[F][J], wh[F][J], 0, 1, 2, 3, 4, 5, 6, 7); \
    acc[J] = (J) >= 3 - NV ? MFMA(xf[F], w_, acc[J]) : MFMA(w_, xf[F], acc[J]); } while (0)
#define QC(F, J) do { csum[J] = __builtin_amdgcn_mfma_f32_4x4x4bf16_1k(ones, wl[F][J], csum[J], 0, 0, 0); } while (0)
#define QD(F, J) do { csum[J] = __builtin_amdgcn_mfma_f32_4x4x4bf16_1k(ones, wh[F][J], csum[J], 0, 0, 0); } while (0)
#define QCS(F) do { QC(F, 0); QC(F, 1); QC(F, 2); QD(F, 0); QD(F, 1); QD(F, 2); SBAR(); } while (0)
#define QHALF(H, S, N, P) do { const unsigned xs = (((H) >> 1) % 3) * 16384, hr = ((H) % 5) * 6144; lds_bar(); SBAR(); \
    if ((H) == 15) { _Pragma("unroll") for (int j_ = 0; j_ < 3; ++j_) cso[j_] = lds_ld<float>((aC ^ 1024) + j_ * 256); \
                     mean = lds_ld<float>(lds0 + Q_STAT + (32 * rt + r) * 4); QCS(P); } \
    if ((H) > 0) { QCS(0); QM(0, 0); SBAR(); } QRX(N, (S) + 1); QRL(N, 0); QRH(N, 0); QRL(0, 0); SBAR(); \
    if ((H) > 0) { QM(0, 1); SBAR(); } QRH(0, 0); QRL(N, 1); QRH(N, 1); QRL(0, 1); SBAR(); \
    if ((H) > 0) { QM(0, 2); SBAR(); } QRH(0, 1); QRX(0, S); QRL(0, 2); SBAR(); \
    if ((H) > 0) { QM(P, 0); SBAR(); } QRH(0, 2); QRL(N, 2); QRH(N, 2); SBAR(); \
    if ((H) > 0) { QM(P, 1); QM(P, 2); SBAR(); } \
    if ((H) == 14) { _Pragma("unroll") for (int j_ = 0; j_ < 3; ++j_) lds_st<float>(aC + j_ * 256, csum[j_][0]); } } while (0)
  const unsigned aC = lds0 + Q_CS + rt * 1024 + lane * 4;
  float cso[3], mean;
  const int tt = rb * 4 + rt;
  unsigned obase[3], ostep[3];
  float osc[3];
#pragma unroll
  for (int j = 0; j < 3; ++j) {
    const int ct = 3 * cb + j;
    const bool isq = ct < 16;
    if (j < 3 - NV) {
      obase[j] = isq ? WS_Q + (unsigned)((2 * ct * 64 + tt) * 1024) : WS_KV + (unsigned)((2 * (ct - 16) * 64 + tt) * 2048);
      ostep[j] = obase[j] + (isq ? 65536u : 131072u);
    } else {
      obase[j] = WS_KV + (unsigned)((2 * (ct - 32) * 64 + tt) * 2048 + 1024);
      ostep[j] = obase[j] + 512u;
    }
    osc[j] = isq ? QSCALE : 1.0f;
    asm volatile("" : "+s"(obase[j]), "+s"(ostep[j]), "+s"(osc[j]));
  }
  const srd_t sws = make_srd(ws, WS_END);
  STAMP(2);
#pragma unroll
  for (int st = 0; st < 8; ++st) {
    QHALF(2 * st, 0, 1, 2);
    QHALF(2 * st + 1, 2, 2, 1);
    if (st == 5) STAMP(8);
  }
  QCS(0); QCS(2);
  QM(0, 0); QM(0, 1); QM(0, 2); QM(2, 0); QM(2, 1); QM(2, 2);
  SBAR();
  float ra = 0.f;
  f32x4 rv[4];
  if (NV < 3) ra = lds_ld<float>(lds0 + Q_STAT + 512 + (32 * rt + r) * 4);
  if (NV > 0) {
#pragma unroll
    for (int g = 0; g < 4; ++g) rv[g] = lds_ld<f32x4>(lds0 + Q_STAT + 512 + (32 * rt + 8 * g + 4 * hh) * 4);
  }
  SBAR();
#undef QHALF
#undef QCS
#undef QD
#undef QC
#undef QM
#undef QRL
#undef QRH
#undef QRX
#undef TRD
  STAMP(3);
  const u32x4 mf = {pk_bf16(-mean, -mean), pk_bf16(-mean, 0.f), 0u, 0u};
#pragma unroll
  for (int j = 0; j < 3; ++j) {
    const float c = csum[j][0] + cso[j], c0 = rbf(c), c1 = rbf(c - c0);
    const u32x4 cf = {pk_bf16(c0, c1), pk_bf16((c - c0) - c1, 0.f), 0u, 0u};
    acc[j] = j >= 3 - NV ? MFMA(__builtin_bit_cast(s16x8, mf), __builtin_bit_cast(s16x8, cf), acc[j]) : MFMA(__builtin_bit_cast(s16x8, cf), __builtin_bit_cast(s16x8, mf), acc[j]);
  }
  STAMP(9);
#pragma unroll
  for (int j = 0; j < 3; ++j) {
    const float a = ra * osc[j];
#pragma unroll
    for (int i = 0; i < 16; ++i) acc[j][i] *= j >= 3 - NV ? rv[i >> 2][i & 3] : a;
#pragma unroll
    for (int e = 0; e < 2; ++e)
      __builtin_amdgcn_raw_buffer_store_b128(u32x4{pk_bf16(acc[j][8 * e], acc[j][8 * e + 1]), pk_bf16(acc[j][8 * e + 2], acc[j][8 * e + 3]),
                                                   pk_bf16(acc[j][8 * e + 4], acc[j][8 * e + 5]), pk_bf16(acc[j][8 * e + 6], acc[j][8 * e + 7])},
                                             sws, j >= 3 - NV ? (unsigned)((r >> 4) * 131072 + hh * 256 + (r & 15) * 16) : (unsigned)(lane * 16), e ? ostep[j] : obase[j], 16);
    SBAR();
  }
  STAMP(10);
#pragma unroll
  for (int j = 0; j < 3 - NV; ++j) {
    float n2[2];
#pragma unroll
    for (int e = 0; e < 2; ++e) {
      float q0 = acc[j][8 * e] * acc[j][8 * e], q1 = acc[j][8 * e + 1] * acc[j][8 * e + 1];
#pragma unroll
      for (int i = 2; i < 8; i += 2) { q0 = __builtin_fmaf(acc[j][8 * e + i], acc[j][8 * e + i], q0); q1 = __builtin_fmaf(acc[j][8 * e + i + 1], acc[j][8 * e + i + 1], q1); }
      n2[e] = q0 + q1;
    }
    auto sw2 = __builtin_amdgcn_permlane32_swap(__float_as_uint(n2[0]), __float_as_uint(n2[1]), false, false);
    const float kn = red32<true>(__uint_as_float(sw2[0]) + __uint_as_float(sw2[1]));
    __builtin_amdgcn_raw_buffer_store_b32(__float_as_uint(kn), sws, r == 0 ? (unsigned)(hh * 256) : OOB, WS_KN + (2 * (3 * cb + j) * 64 + tt) * 4, 16);
  }
}

constexpr int AT_TILE = 2080, AT_SLOT = 8 * AT_TILE, AT_EX = 3 * AT_SLOT;

#define EXP1(r) "v_exp_f32 v" #r ", v" #r "\n\t"
#define CVT1(d, a, b) "v_cvt_pk_bf16_f32 v" #d ", v" #a ", v" #b "\n\t"
#define MF(d, a, b, c) "v_mfma_f32_32x32x16_bf16 " d ", " a ", " b ", " c "\n\t"
#define R_OA "v[16:31]"
#define R_OB "v[32:47]"
#define R_SA "v[48:63]"
#define R_SB "v[64:79]"
#define R_C "v[80:95]"
#define R_QA "v[96:99]"
#define R_QB "v[100:103]"
#define R_K "v[104:107]"
#define R_V0 "v[108:111]"
#define R_V1 "v[112:115]"
#define R_P0 "v[116:119]"
#define R_P1 "v[120:123]"
#define PRHI ""
#define PRLO ""
#define XBAR "s_waitcnt vmcnt(0)\n\ts_barrier\n\t"
#define XDMA(x) x
#define XLDS(x) x
#define XALIGN ".p2align 6\n\t"
#define UNIT_A(PRE, KOFF, VOFF)                                                                                      \
  PRHI MF(R_SB, R_K, R_QB, R_C) EXP1(48) PRE XLDS("ds_read_b128 " R_K ", %[aK] offset:" KOFF "\n\t")                       \
  EXP1(49) EXP1(50) EXP1(51) EXP1(52) MF(R_OB, R_V1, R_P1, R_OB) EXP1(53)                                            \
  XLDS("ds_read_b128 " R_V1 ", %[aV1] offset:" VOFF "\n\t") EXP1(54) EXP1(55) EXP1(56)                                     \
  CVT1(116, 48, 49) CVT1(117, 50, 51) CVT1(118, 52, 53) CVT1(119, 54, 55) EXP1(57)                                   \
  "s_waitcnt lgkmcnt(2)\n\t" EXP1(58) PRLO MF(R_OA, R_V0, R_P0, R_OA) EXP1(59) EXP1(60) EXP1(61) EXP1(62)            \
  "s_waitcnt lgkmcnt(0)\n\t" EXP1(63)                                                                                \
  CVT1(120, 56, 57) CVT1(121, 58, 59) CVT1(122, 60, 61) CVT1(123, 62, 63)
#define UNIT_B(PRE, VOFF)                                                                                            \
  PRHI MF(R_SA, R_K, R_QA, R_C) EXP1(64) EXP1(65) EXP1(66) EXP1(67) EXP1(68) MF(R_OA, R_V1, R_P1, R_OA)              \
  EXP1(69) EXP1(70) EXP1(71) EXP1(72)                                                                                \
  CVT1(116, 64, 65) CVT1(117, 66, 67) CVT1(118, 68, 69) CVT1(119, 70, 71) EXP1(73) EXP1(74)                          \
  PRLO MF(R_OB, R_V0, R_P0, R_OB) EXP1(75) PRE XLDS("ds_read_b128 " R_V0 ", %[aV0] offset:" VOFF "\n\t")                   \
  EXP1(76) EXP1(77) EXP1(78) EXP1(79)                                                                                \
  CVT1(120, 72, 73) CVT1(121, 74, 75) CVT1(122, 76, 77) CVT1(123, 78, 79)

DI void attn_phase(char* ws, unsigned lds0, int b, int w, u64 tag STAMP_ARG) {
  const int lane = TID & 63, r = lane & 31, hh = lane >> 5, qp = w & 3, kh = w >> 2;
  const int head = (b & 7) * 4 + ((b >> 3) & 3), qb = b >> 5;
  if (TID >= 64 && TID < 112) {
    const unsigned i = TID - 64, v = (i & 1) ? 0u : 0x3f803f80u;
    lds_st<u32x4>(lds0 + (i >> 1) * AT_TILE + 2048 + (i & 1) * 16, u32x4{v, v, v, v});
  }
  if (w == 0) {
    const int hp = head >> 1;
    const int f = lane < 16 ? ((16 + hp) / 3) * 16 + lane : lane < 32 ? ((32 + hp) / 3) * 16 + (lane - 16) : (hp / 3) * 16 + 2 * qb + (lane & 1);
    poll(ws, WS_FQ + f * 8, tag);
  }
  lds_bar();
  __builtin_amdgcn_s_setprio(2);
  if (HINT_AT == 2 && TID == 0) flag_store(ws, WS_FS + b * 8, tag);
  STAMP(5);
  const srd_t skv = make_srd(ws + WS_KV + head * 131072, 131072), sws = make_srd(ws, WS_END);
  const unsigned voff = lane * 16, sbase = (unsigned)((32 * kh + qp) * 2048), mbase = lds0 + (unsigned)(w * AT_TILE);
  auto issue = [&](int c, int slot) {
    asm volatile("s_mov_b32 m0, %0\n\ts_nop 0\n\tbuffer_load_dwordx4 %1, %2, %3 offen sc1 lds\n\tbuffer_load_dwordx4 %1, %2, %3 offen offset:1024 sc1 lds"
                 ::"s"(mbase + (unsigned)(slot * AT_SLOT)), "v"(voff), "s"(skv), "s"(sbase + (unsigned)(c * 8192)) : "memory");
  };
  const int qt = qb * 8 + qp * 2;
  const u32x4 qa = __builtin_amdgcn_raw_buffer_load_b128(sws, voff, WS_Q + (head * 64 + qt) * 1024, 16);
  const u32x4 qbf = __builtin_amdgcn_raw_buffer_load_b128(sws, voff, WS_Q + (head * 64 + qt + 1) * 1024, 16);
  const float kn = __uint_as_float(__builtin_amdgcn_raw_buffer_load_b32(sws, lane * 4, WS_KN + (32 + head) * 256, 16));
  const u32x2 qn = __builtin_amdgcn_raw_buffer_load_b64(sws, 0, WS_KN + head * 256 + qt * 4, 16);
  issue(0, 0);
  const float bound = __builtin_amdgcn_sqrtf(comb<true>(__uint_as_float(qn[0]), __uint_as_float(qn[1])) * red_x32<true>(red32<true>(kn))) * 1.01f;
  const bool safe = all_lanes(bound <= BOUND_LIMIT);
  issue(1, 1);
  unsigned aK = lds0 + (unsigned)(kh * 4 * AT_TILE + lane * 16);
  const unsigned cst = (unsigned)(2048 + ((r == 16 || r == 20) ? 0 : 16));
  unsigned aV0 = lds0 + (unsigned)(kh * 4 * AT_TILE) + (r < 16 ? (unsigned)(1024 + hh * 256 + r * 16) : cst);
  unsigned aV1 = lds0 + (unsigned)(kh * 4 * AT_TILE) + (r < 16 ? (unsigned)(1536 + hh * 256 + r * 16) : cst);
  f32x16 oa = {}, ob = {};
  float ma = bound, mb = bound;
  wait_bar<2>();
  STAMP(6);
  if (safe) {
    f32x16 cneg;
#pragma unroll
    for (int i = 0; i < 16; ++i) cneg[i] = -bound;
    unsigned cnt = 0, delta = AT_SLOT, m0n = mbase + 2 * AT_SLOT, soff = sbase + 2 * 8192, ph = 0;
    asm volatile(
        "ds_read_b128 " R_K ", %[aK]\n\t"
        "ds_read_b128 " R_V0 ", %[aV0]\n\t"
        "v_mov_b32 v112, 0\n\tv_mov_b32 v113, 0\n\tv_mov_b32 v114, 0\n\tv_mov_b32 v115, 0\n\t"
        "v_mov_b32 v120, 0\n\tv_mov_b32 v121, 0\n\tv_mov_b32 v122, 0\n\tv_mov_b32 v123, 0\n\t"
        "s_waitcnt lgkmcnt(0)\n\t" MF(R_SA, R_K, R_QA, R_C)
        "s_nop 15\n\t"
        XALIGN
        "1:\n\t"
        UNIT_A("", "2080", "0") UNIT_B("", "2080")
        UNIT_A("", "4160", "2080") UNIT_B("", "4160")
        UNIT_A("", "6240", "4160") UNIT_B("", "6240")
        UNIT_A(XBAR "v_add_u32 %[aK], %[delta], %[aK]\n\t", "0", "6240")
        "s_mov_b32 m0, %[m0n]\n\t"
        UNIT_B(XDMA("buffer_load_dwordx4 %[voff], %[skv], %[soff] offen sc1 lds\n\t")
               "v_add_u32 %[aV0], %[delta], %[aV0]\n\t"
               XDMA("buffer_load_dwordx4 %[voff], %[skv], %[soff] offen offset:1024 sc1 lds\n\t"), "0")
        "v_add_u32 %[aV1], %[delta], %[aV1]\n\t"
        "s_add_u32 %[ph], %[ph], 1\n\t"
        "s_cmp_eq_u32 %[ph], 3\n\t"
        "s_cselect_b32 %[ph], 0, %[ph]\n\t"
        "s_cmp_eq_u32 %[ph], 2\n\t"
        "s_cselect_b32 %[delta], %[cm2], %[cp1]\n\t"
        "s_cmp_eq_u32 %[ph], 1\n\t"
        "s_cselect_b32 m0, %[cm2], %[cp1]\n\t"
        "s_add_u32 %[m0n], %[m0n], m0\n\t"
        "s_add_u32 %[soff], %[soff], 8192\n\t"
        "s_cmp_gt_u32 %[cnt], 4\n\t"
        "s_cselect_b32 %[soff], %[oob], %[soff]\n\t"
        "s_add_u32 %[cnt], %[cnt], 1\n\t"
        "s_cmp_lt_u32 %[cnt], 8\n\t"
        "s_cbranch_scc1 1b\n\t"
        MF(R_OB, R_V1, R_P1, R_OB)
        "s_waitcnt vmcnt(0) lgkmcnt(0)\n\ts_nop 15\n\t"
        : "+{v[16:31]}"(oa), "+{v[32:47]}"(ob), [aK] "+&v"(aK), [aV0] "+&v"(aV0), [aV1] "+&v"(aV1), [cnt] "+&s"(cnt), [ph] "+&s"(ph),
          [delta] "+&s"(delta), [m0n] "+&s"(m0n), [soff] "+&s"(soff)
        : "{v[80:95]}"(cneg), "{v[96:99]}"(qa), "{v[100:103]}"(qbf), [voff] "v"(voff), [skv] "s"(skv),
          [cp1] "s"(AT_SLOT), [cm2] "s"(-2 * AT_SLOT), [oob] "s"(OOB)
        : "memory", "scc", "v48", "v49", "v50", "v51", "v52", "v53", "v54", "v55", "v56", "v57", "v58", "v59", "v60", "v61", "v62", "v63", "v64",
          "v65", "v66", "v67", "v68", "v69", "v70", "v71", "v72", "v73", "v74", "v75", "v76", "v77", "v78", "v79", "v104", "v105", "v106",
          "v107", "v108", "v109", "v110", "v111", "v112", "v113", "v114", "v115", "v116", "v117", "v118", "v119", "v120", "v121", "v122", "v123");
  } else {
    ma = -INFINITY; mb = -INFINITY;
    const s16x8 qf[2] = {__builtin_bit_cast(s16x8, qa), __builtin_bit_cast(s16x8, qbf)};
    int slot = 0;
#pragma unroll 1
    for (int c = 0; c < 8; ++c) {
#pragma unroll 1
      for (int jt = 0; jt < 4; ++jt) {
        const unsigned so = (unsigned)(slot * AT_SLOT + jt * AT_TILE);
        const s16x8 kf = lds_frag(aK + so), v0 = lds_frag(aV0 + so), v1 = lds_frag(aV1 + so);
#pragma unroll
        for (int qi = 0; qi < 2; ++qi) {
          f32x16& o = qi ? ob : oa;
          float& m = qi ? mb : ma;
          f32x16 s = MFMA(kf, qf[qi], f32x16{});
          float tm = s[0];
#pragma unroll
          for (int i = 1; i < 16; ++i) tm = fmaxf(tm, s[i]);
          auto sw2 = __builtin_amdgcn_permlane32_swap(__float_as_uint(tm), __float_as_uint(tm), false, false);
          const float mn = fmaxf(m, fmaxf(__uint_as_float(sw2[0]), __uint_as_float(sw2[1])));
          const float f = __builtin_amdgcn_exp2f(m - mn);
          m = mn;
#pragma unroll
          for (int i = 0; i < 16; ++i) { o[i] *= f; s[i] = __builtin_amdgcn_exp2f(s[i] - mn); }
          const u32x4 p0 = {pk_bf16(s[0], s[1]), pk_bf16(s[2], s[3]), pk_bf16(s[4], s[5]), pk_bf16(s[6], s[7])};
          const u32x4 p1 = {pk_bf16(s[8], s[9]), pk_bf16(s[10], s[11]), pk_bf16(s[12], s[13]), pk_bf16(s[14], s[15])};
          o = MFMA(v0, __builtin_bit_cast(s16x8, p0), o);
          o = MFMA(v1, __builtin_bit_cast(s16x8, p1), o);
        }
      }
      wait_bar<0>();
      if (c < 6) issue(c + 2, slot == 0 ? 2 : slot - 1);
      slot = slot == 2 ? 0 : slot + 1;
    }
  }
  STAMP(7);
  {
    const unsigned ex = lds0 + AT_EX + w * 2560;
    const f32x16& snd = kh ? oa : ob;
    lds_st<f32x4>(ex + lane * 16, f32x4{snd[0], snd[1], snd[2], snd[3]});
    lds_st<f32x4>(ex + 1024 + lane * 16, f32x4{snd[4], snd[5], snd[6], snd[7]});
    lds_st<f32x2>(ex + 2048 + lane * 8, f32x2{snd[8], kh ? ma : mb});
  }
  lds_bar();
  {
    const unsigned ex = lds0 + AT_EX + (w ^ 4) * 2560;
    const f32x16& own = kh ? ob : oa;
    const float mo = kh ? mb : ma;
    const f32x4 t0 = lds_ld<f32x4>(ex + lane * 16), t1 = lds_ld<f32x4>(ex + 1024 + lane * 16);
    const f32x2 t2 = lds_ld<f32x2>(ex + 2048 + lane * 8);
    const float mm = fmaxf(mo, t2[1]);
    const float f0 = __builtin_amdgcn_exp2f(mo - mm), f1 = __builtin_amdgcn_exp2f(t2[1] - mm);
    const float inv = __builtin_amdgcn_rcpf(own[8] * f0 + t2[0] * f1);
    const float g0 = f0 * inv, g1 = f1 * inv;
    const u32x4 pk = {pk_bf16(own[0] * g0 + t0[0] * g1, own[1] * g0 + t0[1] * g1), pk_bf16(own[2] * g0 + t0[2] * g1, own[3] * g0 + t0[3] * g1),
                      pk_bf16(own[4] * g0 + t1[0] * g1, own[5] * g0 + t1[1] * g1), pk_bf16(own[6] * g0 + t1[2] * g1, own[7] * g0 + t1[3] * g1)};
    st16c(sws, WS_O + (unsigned)(((qt + kh) * 32 + head) * 1024) + voff, pk);
  }
  publish(ws, WS_FO + (qb * 32 + head) * 8, tag);
}

struct Geglu {
  float a, d;
  DI Geglu(float d_) : a(__builtin_amdgcn_rcpf(d_)), d(d_) {}
  DI float operator()(float G, float U) const {
    const float c1 = -2.0f * 0.7978845608028654f * 1.4426950408889634f, c3 = c1 * 0.044715f;
    const float g = G * a, e = __builtin_amdgcn_exp2f(g * __builtin_fmaf(g * g, c3, c1));
    return (g * U) * __builtin_amdgcn_rcpf(__builtin_fmaf(e, d, d));
  }
};

DI void up_tile(int j, int& tb, int& fb) {
  const int xcd = j & 7, i = j >> 3;
  const int u = (xcd & 1) * 28 + i;
  tb = 8 * (xcd >> 2) + (u & 7); fb = 7 * ((xcd >> 1) & 1) + (u >> 3);
}

struct UpDma {
  srd_t sx, sg, su;
  unsigned voX, voW, m0, rot;
  DI UpDma(const char* x, const char* wg, const char* wu, unsigned lds0, int j, int w) {
    const int lane = TID & 63, i = j >> 3;
    int tb, fb;
    up_tile(j, tb, fb);
    sx = make_srd(x, SEQ * DM * 2); sg = make_srd(wg, DM * DF * 2); su = make_srd(wu, DM * DF * 2);
    voX = (unsigned)((tb * 128 + 8 * w + (lane >> 3)) * 1024 + (((lane & 7) ^ ((4 * w + (lane >> 4)) & 7)) * 16));
    voW = (unsigned)((4 * w + (lane >> 4)) * (DF * 2) + fb * 256 + (((lane & 15) ^ (((lane >> 4) & 3) << 2)) * 16));
    m0 = lds0 + (unsigned)w * 1024u;
    rot = ROTU * (i & 3);
  }
  DI void X(int st) const { const unsigned k = ((st + rot) & 7) * 128; dma16(m0 + (st & 1) * 16384, voX, sx, k); dma16(m0 + (st & 1) * 16384 + 8192, voX, sx, k + 65536); }
  DI void W(int h) const { const unsigned k = ((h + 2 * rot) & 15) * (32 * DF * 2); dma16(m0 + 32768 + (h % 3) * 16384, voW, sg, k); dma16(m0 + 32768 + (h % 3) * 16384 + 8192, voW, su, k); }
};

DI void ln_phase(const char* x, const char* wqkv, char*& ws, unsigned lds0, int j, int w, unsigned& epoch) {
  const int lane = TID & 63;
  u32x4 xv;
  asm volatile("global_load_dwordx4 %0, %1, %2" : "=v"(xv) : "v"(lane * 16), "s"(x + (8 * j + w) * 1024) : "memory");
  epoch = get_epoch(ws);
  if (PREFETCH && w < 5) asm volatile("s_waitcnt vmcnt(1)" : "+v"(xv)::"memory"); else asm volatile("s_waitcnt vmcnt(0)" : "+v"(xv)::"memory");
  float f[8];
#pragma unroll
  for (int i = 0; i < 4; ++i) { f[2 * i] = bf_lo(xv[i]); f[2 * i + 1] = bf_hi(xv[i]); }
  const float mean = red_x32<false>(red32<false>(((f[0] + f[1]) + (f[2] + f[3])) + ((f[4] + f[5]) + (f[6] + f[7])))) * (1.0f / DM);
  float q = 0.f;
#pragma unroll
  for (int i = 0; i < 8; ++i) q = __builtin_fmaf(f[i] - mean, f[i] - mean, q);
  const float var = red_x32<false>(red32<false>(q)) * (1.0f / DM);
  const float m = rbf(mean), dd = rbf(__builtin_amdgcn_sqrtf(rbf(rbf(var) + 1e-5f)));
  if (lane == 0) flag_store(ws, WS_GS + (8 * j + w) * 8, ((u64)~epoch << 32) | __float_as_uint(m) | (__float_as_uint(dd) >> 16));
  asm volatile("s_barrier" ::: "memory");
  if (TID == 0) flag_store(ws, WS_FL + j * 8, make_tag(epoch));
  {
    unsigned lo, hi, len;
    asm volatile("s_getpc_b64 vcc\n\t.Lpf_here:\n\ts_sub_u32 %0, vcc_lo, .Lpf_here-" KNAME "\n\ts_subb_u32 %1, vcc_hi, 0\n\ts_mov_b32 %2, fused_block_end-" KNAME
                 : "=s"(lo), "=s"(hi), "=s"(len)::"vcc", "scc");
    if (j < 8) touch(lds0 + 65536 + w * 256, lane * 128, make_srd((const void*)(((u64)hi << 32) | lo), len), (unsigned)(w * 8192));
  }
}

DI void up_phase(const char* x, const char* wg, const char* wu, char* ws, unsigned lds0, int j, int w, unsigned epoch STAMP_ARG) {
  const int lane = TID & 63, r = lane & 31, hh = lane >> 5, tp = w >> 2, ft = w & 3;
  const int i = j >> 3;
  int tb, fb;
  up_tile(j, tb, fb);
  const u64 tag = make_tag(epoch);
  const UpDma dma(x, wg, wu, lds0, j, w);
  if (HWAIT == 0) { if (w == 0) poll(ws, WS_FS + j * 8, tag); lds_bar(); }
  __builtin_amdgcn_s_setprio(UPRIO);
  dma.W(0); dma.X(0); dma.W(1);
  STAMP(2);
  const int q = (lane & 15) >> 2, swz = (r >> 1) & 7;
  unsigned aX[4];
#pragma unroll
  for (int s = 0; s < 4; ++s) aX[s] = lds0 + (unsigned)((64 * tp + r) * 128 + (((2 * (s ^ tp) + hh) ^ swz) * 16));
  const unsigned aGb = lds0 + 32768 + (unsigned)((8 * hh + q) * 256 + ((ft ^ q) * 64) + ((lane >> 4) & 1) * 32 + (lane & 3) * 8);
  const unsigned aG[2] = {aGb + tp * 4096, aGb + (tp ^ 1) * 4096};
  const unsigned aC = lds0 + lane * 4;
  const srd_t sws = make_srd(ws, WS_END);
  f32x16 ga = {}, gb = {}, ua = {}, ub = {};
  f32x4 cg = {}, cu = {};
  s16x4 ones = {0x3f80, 0x3f80, 0x3f80, 0x3f80};
  asm volatile("" : "+v"(ones));
  s16x8 xa[2], xb[2];
  s16x4 gl[2], gh[2], ul[2], uh[2];
  u32x2 g0 = {}, g1 = {};
  float og = 0.f, ou = 0.f;
#define TRD(A) __builtin_amdgcn_ds_read_tr16_b64_v4i16((LDSP s16x4*)(uintptr_t)(A))
#define CAT(L, H) __builtin_shufflevector(L, H, 0, 1, 2, 3, 4, 5, 6, 7)
#define URX(E, H) do { const unsigned a_ = aX[2 * ((H) & 1) + (E)] + (((H) >> 1) & 1) * 16384; xa[E] = lds_frag(a_); xb[E] = lds_frag(a_ + 4096); } while (0)
#define URG(E, H) do { const unsigned a_ = aG[E] + ((H) % 3) * 16384; gl[E] = TRD(a_); gh[E] = TRD(a_ + 1024); } while (0)
#define URU(E, H) do { const unsigned a_ = aG[E] + ((H) % 3) * 16384 + 8192; ul[E] = TRD(a_); uh[E] = TRD(a_ + 1024); } while (0)
#define UCS(E) do { cg = __builtin_amdgcn_mfma_f32_4x4x4bf16_1k(ones, gl[E], cg, 0, 0, 0); cu = __builtin_amdgcn_mfma_f32_4x4x4bf16_1k(ones, ul[E], cu, 0, 0, 0); \
    cg = __builtin_amdgcn_mfma_f32_4x4x4bf16_1k(ones, gh[E], cg, 0, 0, 0); cu = __builtin_amdgcn_mfma_f32_4x4x4bf16_1k(ones, uh[E], cu, 0, 0, 0); SBAR(); } while (0)
#define UMM(P, N, H, RD) do { ga = MFMA(CAT(gl[P], gh[P]), xa[P], ga); SBAR(); if (RD) URX(N, H); SBAR(); gb = MFMA(CAT(gl[P], gh[P]), xb[P], gb); SBAR(); \
    if (RD) URG(N, H); SBAR(); ua = MFMA(CAT(ul[P], uh[P]), xa[P], ua); SBAR(); if (RD) URU(N, H); SBAR(); ub = MFMA(CAT(ul[P], uh[P]), xb[P], ub); SBAR(); } while (0)
  wait_bar<2>();
  URX(0, 0); URG(0, 0); URU(0, 0); SBAR();
  dma.W(2); dma.X(1);
#pragma unroll
  for (int h = 0; h < 16; ++h) {
    if (h == 15) { og = lds_ld<float>(aC + (w ^ 4) * 512); ou = lds_ld<float>(aC + (w ^ 4) * 512 + 256); }
    UCS(0);
    UMM(0, 1, h, true);
    if (h < 15) {
      if (h + 1 == HWAIT && w == 0) poll(ws, WS_FS + j * 8, tag);
      if (h == 14) wait_bar<0>(); else if (h & 1) wait_bar<2>(); else wait_bar<4>();
    }
    if (h == 13) { lds_st<float>(aC + w * 512, cg[0]); lds_st<float>(aC + w * 512 + 256, cu[0]); }
    if (h >= 14) UCS(1);
    UMM(1, 0, h + 1, h < 15);
    if (h + 3 < 16) dma.W(h + 3);
    if ((h & 1) && (h + 3) / 2 < 8) dma.X((h + 3) / 2);
    if (h == UPRIO_H) __builtin_amdgcn_s_setprio(0);
    if (h == 3) STAMP(9);
    if (h == 7) STAMP(10);
    if (h == 11) STAMP(6);
    if (h == 12) { g0 = __builtin_amdgcn_raw_buffer_load_b64(sws, r * 8, WS_GS + (tb * 128 + 64 * tp) * 8, 16); g1 = __builtin_amdgcn_raw_buffer_load_b64(sws, r * 8, WS_GS + (tb * 128 + 64 * tp + 32) * 8, 16); }
  }
#undef UMM
#undef UCS
#undef URU
#undef URG
#undef URX
#undef CAT
#undef TRD
  STAMP(3);
  __builtin_amdgcn_s_setprio(3);
#pragma unroll 1
  for (int it = 0; it < (1 << 16) && !all_lanes(g0[1] == ~epoch && g1[1] == ~epoch); ++it) {
    g0 = __builtin_amdgcn_raw_buffer_load_b64(sws, r * 8, WS_GS + (tb * 128 + 64 * tp) * 8, 16);
    g1 = __builtin_amdgcn_raw_buffer_load_b64(sws, r * 8, WS_GS + (tb * 128 + 64 * tp + 32) * 8, 16);
  }
  {
    const float c = cg[0] + og, c0 = rbf(c), c1 = rbf(c - c0), d = cu[0] + ou, d0 = rbf(d), d1 = rbf(d - d0);
    const s16x8 cf = __builtin_bit_cast(s16x8, u32x4{pk_bf16(c0, c1), pk_bf16((c - c0) - c1, 0.f), 0u, 0u});
    const s16x8 df = __builtin_bit_cast(s16x8, u32x4{pk_bf16(d0, d1), pk_bf16((d - d0) - d1, 0.f), 0u, 0u});
    const unsigned n0 = (g0[0] >> 16) ^ 0x8000u, n1 = (g1[0] >> 16) ^ 0x8000u;
    const s16x8 m0 = __builtin_bit_cast(s16x8, u32x4{n0 * 0x10001u, n0, 0u, 0u}), m1 = __builtin_bit_cast(s16x8, u32x4{n1 * 0x10001u, n1, 0u, 0u});
    ga = MFMA(cf, m0, ga); ua = MFMA(df, m0, ua); gb = MFMA(cf, m1, gb); ub = MFMA(df, m1, ub);
  }
  const unsigned ho = WS_H + (unsigned)(((tb * 4 + tp * 2) * 112 + fb * 8 + ft * 2) * 1024) + lane * 16;
#pragma unroll
  for (int t = 0; t < 2; ++t) {
    const f32x16& g = t ? gb : ga;
    const f32x16& u = t ? ub : ua;
    const Geglu act(bf_lo(t ? g1[0] : g0[0]));
#pragma unroll
    for (int e = 0; e < 2; ++e) {
      u32x4 pk;
#pragma unroll
      for (int k = 0; k < 4; ++k) pk[k] = pk_bf16(act(g[8 * e + 2 * k], u[8 * e + 2 * k]), act(g[8 * e + 2 * k + 1], u[8 * e + 2 * k + 1]));
      st16c(sws, ho + t * 112 * 1024 + e * 1024, pk);
    }
  }
  __builtin_amdgcn_s_setprio(0);
  publish(ws, WS_FH + (tb * 16 + fb) * 8, tag);
}

DI void out_phase(const char* x, const char* wd, const char* wo, char* ws, char* out, unsigned lds0, int j, int w, u64 tag STAMP_ARG) {
  const int lane = TID & 63, r = lane & 31, hh = lane >> 5;
  const int xcd = j & 7, i = j >> 3, mb = 4 * xcd + (i & 3), nb = i >> 2;
  const int tl = w & 1, nt = (w >> 1) & 1, kh = w >> 2;
  const unsigned voX = lane * 16, soH = (unsigned)(((2 * mb + (w >> 2)) * 112 + (w & 3)) * 1024);
  const unsigned voW = (unsigned)((8 * w + (lane >> 3)) * 1024 + nb * 128 + (((lane & 7) ^ (((lane >> 4) & 1) << 2)) * 16));
  {
    const srd_t sw = make_srd(wo, DM * DM * 2);
#pragma unroll
    for (int st = 0; st < 8; ++st) dma16(lds0 + st * 8192 + w * 1024, voW, sw, st * 65536);
  }
  const int ocol = 64 * nb + 32 * nt + 16 * kh + 4 * hh, orow = 64 * mb + 32 * tl + r;
  const u32x2 xr0 = *(const u32x2*)(x + orow * 1024 + ocol * 2), xr1 = *(const u32x2*)(x + orow * 1024 + ocol * 2 + 16);
  if (w < 7) touch(lds0 + 65536 + w * 256, lane * 128, make_srd(wd, DF * DM * 2), (unsigned)((i * 7 + w) * 8192));
  if (w == 0) poll(ws, WS_FH + ((mb >> 1) * 16 + (lane < 14 ? lane : 0)) * 8, tag);
  wait_bar<0>();
  const int q = (lane & 15) >> 2;
  const unsigned aX = lds0 + tl * 4096 + kh * 2048 + lane * 16;
  const unsigned aW = lds0 + (unsigned)((32 * kh + 4 * hh + q) * 128 + ((nt ^ (q >> 1)) * 64) + ((lane >> 4) & 1) * 32 + (lane & 3) * 8);
  s16x8 wf[16];
#pragma unroll
  for (int k = 0; k < 16; ++k) wf[k] = lds_tr_frag(aW + (k >> 1) * 8192 + (k & 1) * 2048, aW + (k >> 1) * 8192 + (k & 1) * 2048 + 1024);
  lds_bar();
  STAMP(5);
  const srd_t sh = make_srd(ws + WS_H, 64u * 112u * 1024u), sd = make_srd(wd, DF * DM * 2);
  const int rot = ROTD ? (7 * (i & 3) + 3 * (i >> 2)) % 28 : 0;
  auto issue = [&](int st, unsigned slot) {
    const unsigned sl = lds0 + slot + (unsigned)w * 1024u;
    const unsigned k = (unsigned)(st + rot >= 28 ? st + rot - 28 : st + rot);
    dma16c(sl, voX, sh, st < 28 ? soH + k * 4096 : OOB);
    dma16(sl + 8192, voW, sd, st < 28 ? k * 65536 : OOB);
  };
#pragma unroll
  for (int st = 0; st < 4; ++st) issue(st, st * 16384);
  f32x16 acc = {};
  {
    s16x8 x0, w0, x1, w1;
#define DRD(X, W, SL, E) do { X = lds_frag(aX + (SL) + (E) * 1024); W = lds_tr_frag(aW + 8192 + (SL) + (E) * 2048, aW + 8192 + (SL) + (E) * 2048 + 1024); SBAR(); } while (0)
#define DSTAGE(ST, SL, SN) do { DRD(x1, w1, SL, 1); acc = MFMA(w0, x0, acc); SBAR(); wait_bar<4>(); \
    DRD(x0, w0, SN, 0); acc = MFMA(w1, x1, acc); SBAR(); issue((ST) + 4, SL); } while (0)
    wait_bar<6>();
    DRD(x0, w0, 0, 0);
#pragma unroll 1
    for (int st = 0; st < 28; st += 4) { DSTAGE(st, 0, 16384); DSTAGE(st + 1, 16384, 32768); DSTAGE(st + 2, 32768, 49152); DSTAGE(st + 3, 49152, 0); }
#undef DSTAGE
#undef DRD
  }
  STAMP(7);
  if (w == 0) poll(ws, WS_FO + ((mb >> 2) * 32 + r) * 8, tag);
  wait_bar<0>();
  STAMP(8);
  {
    const srd_t so = make_srd(ws + WS_O, 2u << 20);
    const unsigned soO = (unsigned)(((2 * mb + (w >> 2)) * 32 + (w & 3)) * 1024);
#pragma unroll
    for (int st = 0; st < 8; ++st) dma16c(lds0 + st * 8192 + w * 1024, voX, so, soO + st * 4096);
  }
#pragma unroll
  for (int g = 0; g < 4; ++g) {
    if (g == 0) wait_bar<6>(); else if (g == 1) wait_bar<4>(); else if (g == 2) wait_bar<2>(); else wait_bar<0>();
    s16x8 xf[4];
#pragma unroll
    for (int k = 0; k < 4; ++k) xf[k] = lds_frag(aX + (2 * g + (k >> 1)) * 8192 + (k & 1) * 1024);
#pragma unroll
    for (int k = 0; k < 4; ++k) acc = MFMA(wf[4 * g + k], xf[k], acc);
  }
  {
    const unsigned ex = lds0 + 65536 + w * 2048 + lane * 16;
    lds_st<f32x4>(ex, kh ? f32x4{acc[0], acc[1], acc[2], acc[3]} : f32x4{acc[8], acc[9], acc[10], acc[11]});
    lds_st<f32x4>(ex + 1024, kh ? f32x4{acc[4], acc[5], acc[6], acc[7]} : f32x4{acc[12], acc[13], acc[14], acc[15]});
  }
  lds_bar();
  {
    const unsigned ex = lds0 + 65536 + (w ^ 4) * 2048 + lane * 16;
    const f32x4 t0 = lds_ld<f32x4>(ex), t1 = lds_ld<f32x4>(ex + 1024);
    float v[8];
#pragma unroll
    for (int k = 0; k < 4; ++k) { v[k] = (kh ? acc[8 + k] : acc[k]) + t0[k]; v[4 + k] = (kh ? acc[12 + k] : acc[4 + k]) + t1[k]; }
    const u32x2 o0 = {pk_bf16(v[0] + bf_lo(xr0[0]), v[1] + bf_hi(xr0[0])), pk_bf16(v[2] + bf_lo(xr0[1]), v[3] + bf_hi(xr0[1]))};
    const u32x2 o1 = {pk_bf16(v[4] + bf_lo(xr1[0]), v[5] + bf_hi(xr1[0])), pk_bf16(v[6] + bf_lo(xr1[1]), v[7] + bf_hi(xr1[1]))};
    *(u32x2*)(out + orow * 1024 + ocol * 2) = o0;
    *(u32x2*)(out + orow * 1024 + ocol * 2 + 16) = o1;
  }
}

extern "C" __global__ __launch_bounds__(512, 4) void fused_block(const char* __restrict__ x, const char* __restrict__ wqkv, const char* __restrict__ wo,
                                                      const char* __restrict__ wg, const char* __restrict__ wu, const char* __restrict__ wd,
                                                      char* ws0, char* __restrict__ out) {
  extern __shared__ __attribute__((aligned(16))) char lds[];
  const unsigned lds0 = (unsigned)(uintptr_t)(LDSP char*)lds;
  STAMP_DECL; STAMP(0);
  int w = __builtin_amdgcn_readfirstlane(TID >> 6);
  asm volatile("s_nop 4" : "+s"(w));
  unsigned epoch;
  asm volatile("s_mov_b32 %0, 0" : "=s"(epoch));
  char* ws = ws0;
  const int b = BID & 255, rb = 8 * ((b & 7) >> 2) + ((b >> 3) & 7), cb = 4 * (b & 3) + (b >> 6);
  if (__builtin_expect(BID >= 256 && w >= 4, 1)) {
    __builtin_amdgcn_s_setprio(LPRIO);
    qkv_loader<0>(x, wqkv, ws, lds0, b, rb, cb, w, epoch STAMP_PASS);
  }
  asm volatile("" ::: "memory");
  if (__builtin_expect(BID < 256, 1)) ln_phase(x, wqkv, ws, lds0, b, w, epoch);
  asm volatile("" ::: "memory");
  if (__builtin_expect(BID >= 256 && w >= 4, 1)) qkv_loader<1>(x, wqkv, ws, lds0, b, rb, cb, w, epoch STAMP_PASS);
  if (BID >= 256) {
    if (w < 4) {
      __builtin_amdgcn_s_setprio(2);
      if (cb < 10) qkv_wave<0>(ws, lds0, rb, cb, w, epoch STAMP_PASS);
      else if (cb == 10) qkv_wave<1>(ws, lds0, rb, cb, w, epoch STAMP_PASS);
      else qkv_wave<3>(ws, lds0, rb, cb, w, epoch STAMP_PASS);
    }
    const u64 tag = make_tag(epoch);
    publish(ws, WS_FQ + (cb * 16 + rb) * 8, tag);
    if (HINT_AT == 1 && TID == 0) flag_store(ws, WS_FS + b * 8, tag);
    STAMP(4);
    attn_phase(ws, lds0, b, w, tag STAMP_PASS);
  } else {
    const int j = BID;
    const u64 tag = make_tag(epoch);
    STAMP(1);
    if (j < 224) {
      up_phase(x, wg, wu, ws, lds0, j, w, epoch STAMP_PASS);
    }
    if (j == 255 && w == 1) {
      const int lane = TID & 63;
#pragma unroll 1
      for (int k = 0; k < 8; ++k) poll(ws, WS_FQ + (k * 64 + lane) * 8, tag);
      if (lane == 0) __hip_atomic_store((gu32*)(ws0 + WS_EP), epoch, __ATOMIC_RELAXED, __HIP_MEMORY_SCOPE_AGENT);
    }
    STAMP(4);
    out_phase(x, wd, wo, ws, out, lds0, j, w, tag STAMP_PASS);
  }
  STAMP_FLUSH(ws0);
}

extern "C" __device__ __attribute__((noinline, used, visibility("hidden"))) void fused_block_end() {}

extern "C" void kernel_launch(void* const* d_in, const int* in_sizes, int n_in, void* d_out, int out_size, void* d_ws, size_t ws_size,
                              hipStream_t stream) {
  (void)hipFuncSetAttribute((const void*)fused_block, hipFuncAttributeMaxDynamicSharedMemorySize, LDS_BYTES);
  fused_block<<<512, 512, LDS_BYTES, stream>>>((const char*)d_in[0], (const char*)d_in[1], (const char*)d_in[2], (const char*)d_in[4],
                                               (const char*)d_in[5], (const char*)d_in[6], (char*)d_ws, (char*)d_out);
}

#if defined(__HIP_DEVICE_COMPILE__)
#pragma clang attribute pop
#endif
```

```cpp
#include <hip/hip_runtime.h>
#include <stdint.h>

typedef float f32x16 __attribute__((ext_vector_type(16)));
typedef float f32x4 __attribute__((ext_vector_type(4)));
typedef float f32x2 __attribute__((ext_vector_type(2)));
typedef short s16x8 __attribute__((ext_vector_type(8)));
typedef short s16x4 __attribute__((ext_vector_type(4)));
typedef __bf16 bf16x2 __attribute__((ext_vector_type(2)));
typedef unsigned u32x4 __attribute__((ext_vector_type(4)));
typedef unsigned u32x2 __attribute__((ext_vector_type(2)));
typedef unsigned long long u64;
typedef __attribute__((address_space(1))) u64 gu64;
typedef __attribute__((address_space(1))) unsigned gu32;
typedef __amdgpu_buffer_rsrc_t srd_t;
#define LDSP __attribute__((address_space(3)))
#define DI __device__ __forceinline__
#define MFMA(a, b, c) __builtin_amdgcn_mfma_f32_32x32x16_bf16((a), (b), (c), 0, 0, 0)
#define SBAR() __builtin_amdgcn_sched_barrier(0)
#define TID ((int)__builtin_amdgcn_workitem_id_x())
#define BID ((int)__builtin_amdgcn_workgroup_id_x())
#define LANE_ID() ({ int l_; asm volatile("v_mbcnt_lo_u32_b32 %0, -1, 0\n\tv_mbcnt_hi_u32_b32 %0, -1, %0" : "=v"(l_)); l_; })
#define IS_A (BID < 256)

constexpr int SEQ = 2048, DM = 512, DF = 1792;
constexpr unsigned FST = 128;
constexpr unsigned WS_Q = 0;
constexpr unsigned WS_KV = WS_Q + (2u << 20);
constexpr unsigned WS_O = WS_KV + (4u << 20);
constexpr unsigned WS_H = WS_O + (2u << 20);
constexpr unsigned WS_KN = WS_H + 64u * 112u * 1024u;
constexpr unsigned WS_GS = WS_KN + 32768;
constexpr unsigned WS_GC = WS_GS + 16384;
constexpr unsigned WS_FQ = WS_GC + 28672;
constexpr unsigned WS_FL = WS_FQ + 256 * FST;
constexpr unsigned WS_FH = WS_FL + 256 * FST;
constexpr unsigned WS_FO = WS_FH + 256 * FST;
constexpr unsigned WS_FS = WS_FO + 2048 * FST;
constexpr unsigned WS_END = WS_FS + 256 * FST;
constexpr unsigned WS_EP = WS_END;
constexpr float QSCALE = 0.25f * 1.4426950408889634f;
constexpr float BOUND_LIMIT = 48.0f;
constexpr unsigned OOB = 0x40000000u;
constexpr int LDS_BYTES = 81920;

DI bool all_lanes(bool c) { return __builtin_amdgcn_ballot_w64(!c) == 0; }
DI srd_t make_srd(const void* p, unsigned bytes) { return __builtin_amdgcn_make_buffer_rsrc((void*)p, (short)0, (int)bytes, 0x00020000); }
DI unsigned pk_bf16(float lo, float hi) { f32x2 v = {lo, hi}; bf16x2 b = __builtin_convertvector(v, bf16x2); return __builtin_bit_cast(unsigned, b); }
DI float rbf(float v) { return __uint_as_float(pk_bf16(v, v) << 16); }
DI float mul1(float a, float b) { float r; asm("v_mul_f32 %0, %1, %2" : "=v"(r) : "v"(a), "v"(b)); return r; }
DI float fma1(float a, float b, float c) { float r; asm("v_fma_f32 %0, %1, %2, %3" : "=v"(r) : "v"(a), "v"(b), "v"(c)); return r; }
DI float bf_lo(unsigned u) { return __uint_as_float(u << 16); }
DI float bf_hi(unsigned u) { return __uint_as_float(u & 0xffff0000u); }
template <bool MAX> DI float comb(float a, float b) { return MAX ? __uint_as_float(__builtin_elementwise_max(__float_as_uint(a), __float_as_uint(b))) : a + b; }
template <bool MAX, int CTRL> DI float red_dpp(float v) { return comb<MAX>(v, __int_as_float(__builtin_amdgcn_update_dpp(0, __float_as_int(v), CTRL, 0xf, 0xf, true))); }
template <bool MAX> DI float red_x16(float v) { auto r = __builtin_amdgcn_permlane16_swap(__float_as_uint(v), __float_as_uint(v), false, false); return comb<MAX>(__uint_as_float(r[0]), __uint_as_float(r[1])); }
template <bool MAX> DI float red_x32(float v) { auto r = __builtin_amdgcn_permlane32_swap(__float_as_uint(v), __float_as_uint(v), false, false); return comb<MAX>(__uint_as_float(r[0]), __uint_as_float(r[1])); }
template <bool MAX> DI float red32(float v) {
  v = red_dpp<MAX, 0xB1>(v); v = red_dpp<MAX, 0x4E>(v); v = red_dpp<MAX, 0x141>(v); v = red_dpp<MAX, 0x140>(v); return red_x16<MAX>(v);
}
DI void dma16(unsigned lds, unsigned voff, srd_t srd, unsigned soff) {
  asm volatile("s_mov_b32 m0, %0\n\ts_nop 0\n\tbuffer_load_dwordx4 %1, %2, %3 offen lds" ::"s"(lds), "v"(voff), "s"(srd), "s"(soff) : "memory");
}
DI void dma16c(unsigned lds, unsigned voff, srd_t srd, unsigned soff) {
  asm volatile("s_mov_b32 m0, %0\n\ts_nop 0\n\tbuffer_load_dwordx4 %1, %2, %3 offen sc1 lds" ::"s"(lds), "v"(voff), "s"(srd), "s"(soff) : "memory");
}
DI void touch(unsigned lds, unsigned voff, srd_t srd, unsigned soff) {
  asm volatile("s_mov_b32 m0, %0\n\ts_nop 0\n\tbuffer_load_dword %1, %2, %3 offen lds" ::"s"(lds), "v"(voff), "s"(srd), "s"(soff) : "memory");
}
template <int N> DI void wait_bar() { asm volatile("s_waitcnt vmcnt(%0) lgkmcnt(0)\n\ts_barrier" ::"i"(N) : "memory"); }
DI void lds_bar() { asm volatile("s_waitcnt lgkmcnt(0)\n\ts_barrier" ::: "memory"); }
DI s16x8 lds_frag(unsigned a) { return *(LDSP const s16x8*)(uintptr_t)a; }
DI s16x8 lds_tr_frag(unsigned a0, unsigned a1) {
  s16x4 a = __builtin_amdgcn_ds_read_tr16_b64_v4i16((LDSP s16x4*)(uintptr_t)a0), b = __builtin_amdgcn_ds_read_tr16_b64_v4i16((LDSP s16x4*)(uintptr_t)a1);
  return __builtin_shufflevector(a, b, 0, 1, 2, 3, 4, 5, 6, 7);
}
template <class T> DI T lds_ld(unsigned a) { return *(LDSP const T*)(uintptr_t)a; }
template <class T> DI void lds_st(unsigned a, T v) { *(LDSP T*)(uintptr_t)a = v; }
DI void st16c(srd_t srd, unsigned voff, u32x4 v) { __builtin_amdgcn_raw_buffer_store_b128(v, srd, voff, 0, 16); }
DI u64 make_tag(unsigned e) { return ((u64)(e ^ 0x5bd1e995u) << 32) | e; }
DI unsigned get_epoch(const char* ws) {
  unsigned e;
  asm volatile("s_load_dword %0, %1, %2\n\ts_waitcnt lgkmcnt(0)" : "=s"(e) : "s"(ws), "s"(WS_EP) : "memory");
  return e + 1u;
}
DI void flag_store(char* ws, unsigned off, u64 tag) {
  __hip_atomic_store((gu64*)(ws + off), tag, __ATOMIC_RELAXED, __HIP_MEMORY_SCOPE_AGENT);
}
DI void publish(char* ws, unsigned off, u64 tag, int w) { wait_bar<0>(); if (w == 0 && LANE_ID() == 0) flag_store(ws, off, tag); }
DI void poll(char* ws, unsigned off, u64 tag) {
#pragma unroll 1
  for (int it = 0; it < (1 << 16); ++it) {
    const u64 v = __hip_atomic_load((gu64*)(ws + off), __ATOMIC_RELAXED, __HIP_MEMORY_SCOPE_AGENT);
    if (all_lanes(v == tag)) break;
    __builtin_amdgcn_s_sleep(4);
  }
}

constexpr unsigned Q_W = 49152, Q_STAT = Q_W + 5 * 6144, Q_CS = 32768;

template <int PART>
DI void qkv_loader(const char* x, const char* wqkv, char* ws, unsigned lds0, int b, int rb, int cb, int w, unsigned& epoch) {
  const int lane = LANE_ID(), L = w - 4;
  const srd_t sx = make_srd(x, SEQ * DM * 2), sw = make_srd(wqkv, DM * 3 * DM * 2);
  const unsigned voX0 = (unsigned)((rb * 128 + 32 * L + (lane >> 3)) * 1024 + (((lane & 7) ^ (lane >> 4)) * 16));
  const unsigned voX1 = (unsigned)((rb * 128 + 32 * L + 8 + (lane >> 3)) * 1024 + (((lane & 7) ^ (4 + (lane >> 4))) * 16)) - 1024u;
  const unsigned voW0 = (unsigned)((8 * L + lane / 12) * 3072 + cb * 192 + (lane % 12) * 16), voW1 = voW0 + 4 * 3072 - 768;
  auto group = [&](unsigned hslot, unsigned hs, unsigned xslot, unsigned st, int part) {
    asm volatile("s_mov_b32 m0, %0\n\ts_mov_b32 exec_hi, 0xffff\n\tbuffer_load_dwordx4 %1, %3, %4 offen lds\n\tbuffer_load_dwordx4 %2, %3, %4 offen offset:768 lds\n\t"
                 "s_mov_b32 exec_hi, -1"
                 ::"s"(lds0 + Q_W + hslot + L * 1536), "v"(voW0), "v"(voW1), "s"(sw), "s"(hs * 98304) : "memory");
    asm volatile("s_mov_b32 m0, %0\n\ts_nop 0\n\tbuffer_load_dwordx4 %1, %3, %4 offen lds\n\tbuffer_load_dwordx4 %2, %3, %4 offen offset:1024 lds"
                 ::"s"(lds0 + xslot + L * 4096 + part * 2048), "v"(voX0), "v"(voX1), "s"(sx), "s"(st * 128 + part * 16384) : "memory");
  };
  if (PART == 0) {
    __builtin_amdgcn_s_setprio(2);
    group(0, 0, 0, 0, 0); group(6144, 1, 0, 0, 1); group(2 * 6144, 2, 16384, 1, 0); group(3 * 6144, 3, 16384, 1, 1);
    return;
  }
#pragma unroll
  for (int st = 0; st < 6; ++st) {
    wait_bar<8>();
    group(((2 * st + 4) % 5) * 6144, 2 * st + 4, ((st + 2) % 3) * 16384, st + 2, 0);
    wait_bar<12>();
    group(((2 * st + 5) % 5) * 6144, 2 * st + 5, ((st + 2) % 3) * 16384, st + 2, 1);
  }
  epoch = get_epoch(ws);
  wait_bar<8>();
  const srd_t sws = make_srd(ws, WS_END);
  const unsigned go = WS_GS + rb * 1024 + lane * 16;
  u32x4 gs;
  asm volatile("buffer_load_dwordx4 %0, %1, %2, 0 offen sc1" : "=v"(gs) : "v"(go), "s"(sws) : "memory");
  wait_bar<9>();
  asm volatile("s_waitcnt vmcnt(0)\n\ts_barrier" : "+v"(gs)::"memory");
  if (L == 0) {
#pragma unroll 1
    for (int it = 0; it < (1 << 16) && !all_lanes(gs[1] == epoch && gs[3] == epoch); ++it) gs = __builtin_amdgcn_raw_buffer_load_b128(sws, go, 0, 16);
    lds_st<f32x2>(lds0 + Q_STAT + lane * 8, f32x2{bf_hi(gs[0]), bf_hi(gs[2])});
    lds_st<f32x2>(lds0 + Q_STAT + 512 + lane * 8, f32x2{__builtin_amdgcn_rcpf(bf_lo(gs[0])), __builtin_amdgcn_rcpf(bf_lo(gs[2]))});
  }
  lds_bar();
  asm volatile("s_barrier" ::: "memory");
}

template <int NV>
DI void qkv_wave(char* ws, unsigned lds0, int rb, int cb, int rt, unsigned& epoch) {
  __builtin_amdgcn_s_setprio(2);
  const int lane = LANE_ID(), r = lane & 31, hh = lane >> 5;
  const int swz = (r >> 1) & 7, sw = rt & 1;
  unsigned aX[4];
#pragma unroll
  for (int s = 0; s < 4; ++s) aX[s] = lds0 + (unsigned)((32 * rt + r) * 128 + (((2 * (s ^ sw) + hh) ^ swz) * 16));
  const unsigned aWb = lds0 + Q_W + (unsigned)((8 * hh + ((lane & 15) >> 2)) * 192 + ((lane >> 4) & 1) * 32 + (lane & 3) * 8);
  const unsigned aW[2] = {aWb + sw * 3072, aWb + (sw ^ 1) * 3072};
  f32x16 acc[3] = {};
  f32x4 csum[3] = {};
  s16x4 ones = {0x3f80, 0x3f80, 0x3f80, 0x3f80};
  asm volatile("" : "+v"(ones));
  s16x8 xf[3];
  s16x4 wl[3][3], wh[3][3];
#define TRD(A) __builtin_amdgcn_ds_read_tr16_b64_v4i16((LDSP s16x4*)(uintptr_t)(A))
#define QRX(F, S) xf[F] = lds_frag(aX[S] + xs)
#define QRL(F, J) wl[F][J] = TRD(aW[(F) != 0] + hr + 64 * (J))
#define QRH(F, J) wh[F][J] = TRD(aW[(F) != 0] + hr + 768 + 64 * (J))
#define QM(F, J) do { const s16x8 w_ = __builtin_shufflevector(wl[F][J], wh[F][J], 0, 1, 2, 3, 4, 5, 6, 7); \
    acc[J] = (J) >= 3 - NV ? MFMA(xf[F], w_, acc[J]) : MFMA(w_, xf[F], acc[J]); } while (0)
#define QC(F, J) do { csum[J] = __builtin_amdgcn_mfma_f32_4x4x4bf16_1k(ones, wl[F][J], csum[J], 0, 0, 0); } while (0)
#define QD(F, J) do { csum[J] = __builtin_amdgcn_mfma_f32_4x4x4bf16_1k(ones, wh[F][J], csum[J], 0, 0, 0); } while (0)
#define QCS(F) do { QC(F, 0); QC(F, 1); QC(F, 2); QD(F, 0); QD(F, 1); QD(F, 2); SBAR(); } while (0)
#define QHALF(H, S, N, P) do { const unsigned xs = (((H) >> 1) % 3) * 16384, hr = ((H) % 5) * 6144; lds_bar(); SBAR(); \
    if ((H) == 15) { _Pragma("unroll") for (int j_ = 0; j_ < 3; ++j_) cso[j_] = lds_ld<float>(aC + (rt ^ 1) * 1024 + j_ * 256); \
                     mean = lds_ld<float>(lds0 + Q_STAT + (32 * rt + r) * 4); QCS(P); } \
    if ((H) > 0) { QCS(0); QM(0, 0); SBAR(); } QRX(N, (S) + 1); QRL(N, 0); QRH(N, 0); QRL(0, 0); SBAR(); \
    if ((H) > 0) { QM(0, 1); SBAR(); } QRH(0, 0); QRL(N, 1); QRH(N, 1); QRL(0, 1); SBAR(); \
    if ((H) > 0) { QM(0, 2); SBAR(); } QRH(0, 1); QRX(0, S); QRL(0, 2); SBAR(); \
    if ((H) > 0) { QM(P, 0); SBAR(); } QRH(0, 2); QRL(N, 2); QRH(N, 2); SBAR(); \
    if ((H) > 0) { QM(P, 1); QM(P, 2); SBAR(); } \
    if ((H) == 14) { _Pragma("unroll") for (int j_ = 0; j_ < 3; ++j_) lds_st<float>(aC + rt * 1024 + j_ * 256, csum[j_][0]); } } while (0)
  const unsigned aC = lds0 + Q_CS + lane * 4;
  float cso[3], mean;
  const int tt = rb * 4 + rt;
  unsigned obase[3], ostep[3];
  unsigned osc[3];
#pragma unroll
  for (int j = 0; j < 3; ++j) {
    const int ct = 3 * cb + j;
    const bool isq = ct < 16;
    if (j < 3 - NV) {
      obase[j] = isq ? WS_Q + (unsigned)((2 * ct * 64 + tt) * 1024) : WS_KV + (unsigned)((2 * (ct - 16) * 64 + tt) * 2048);
      ostep[j] = obase[j] + (isq ? 65536u : 131072u);
    } else {
      obase[j] = WS_KV + (unsigned)((2 * (ct - 32) * 64 + tt) * 2048 + 1024);
      ostep[j] = obase[j] + 512u;
    }
    osc[j] = __builtin_amdgcn_readfirstlane(isq ? __builtin_bit_cast(unsigned, QSCALE) : 0x3f800000u);
    asm volatile("" : "+s"(obase[j]), "+s"(ostep[j]), "+s"(osc[j]));
  }
#pragma unroll
  for (int st = 0; st < 8; ++st) {
    QHALF(2 * st, 0, 1, 2);
    QHALF(2 * st + 1, 2, 2, 1);
    if (st == 4) { epoch = get_epoch(ws); SBAR(); }
  }
  QCS(0); QCS(2);
  QM(0, 0); QM(0, 1); QM(0, 2);
  SBAR();
  float ra = 0.f;
  f32x4 rv[4];
  if (NV < 3) ra = lds_ld<float>(lds0 + Q_STAT + 512 + (32 * rt + r) * 4);
  if (NV > 0) {
#pragma unroll
    for (int g = 0; g < 4; ++g) rv[g] = lds_ld<f32x4>(lds0 + Q_STAT + 512 + (32 * rt + 8 * g + 4 * hh) * 4);
  }
  const u32x4 mf = {pk_bf16(-mean, -mean), pk_bf16(-mean, 0.f), 0u, 0u};
  u32x4 cf[3];
#pragma unroll
  for (int j = 0; j < 3; ++j) {
    const float c = csum[j][0] + cso[j], c0 = rbf(c), c1 = rbf(c - c0);
    cf[j] = u32x4{pk_bf16(c0, c1), pk_bf16((c - c0) - c1, 0.f), 0u, 0u};
  }
  SBAR();
#pragma unroll
  for (int j = 0; j < 3; ++j) {
    QM(2, j);
    acc[j] = j >= 3 - NV ? MFMA(__builtin_bit_cast(s16x8, mf), __builtin_bit_cast(s16x8, cf[j]), acc[j]) : MFMA(__builtin_bit_cast(s16x8, cf[j]), __builtin_bit_cast(s16x8, mf), acc[j]);
    SBAR();
  }
#undef QHALF
#undef QCS
#undef QD
#undef QC
#undef QM
#undef QRL
#undef QRH
#undef QRX
#undef TRD
  const srd_t sws = make_srd(ws, WS_END);
  asm volatile("s_nop 8" : "+v"(acc[0]), "+v"(acc[1]), "+v"(acc[2]));
#pragma unroll
  for (int j = 0; j < 3; ++j) {
    const float a = ra * __uint_as_float(osc[j]);
#pragma unroll
    for (int i = 0; i < 16; ++i) acc[j][i] = mul1(acc[j][i], j >= 3 - NV ? rv[i >> 2][i & 3] : a);
#pragma unroll
    for (int e = 0; e < 2; ++e)
      __builtin_amdgcn_raw_buffer_store_b128(u32x4{pk_bf16(acc[j][8 * e], acc[j][8 * e + 1]), pk_bf16(acc[j][8 * e + 2], acc[j][8 * e + 3]),
                                                   pk_bf16(acc[j][8 * e + 4], acc[j][8 * e + 5]), pk_bf16(acc[j][8 * e + 6], acc[j][8 * e + 7])},
                                             sws, j >= 3 - NV ? (unsigned)((r >> 4) * 131072 + hh * 256 + (r & 15) * 16) : (unsigned)(lane * 16), e ? ostep[j] : obase[j], 16);
    SBAR();
  }
  publish(ws, WS_FQ + (cb * 16 + rb) * FST, make_tag(epoch), rt);
#pragma unroll
  for (int j = 0; j < 3 - NV; ++j) {
    float n2[2];
#pragma unroll
    for (int e = 0; e < 2; ++e) {
      float q0 = mul1(acc[j][8 * e], acc[j][8 * e]), q1 = mul1(acc[j][8 * e + 1], acc[j][8 * e + 1]);
#pragma unroll
      for (int i = 2; i < 8; i += 2) { q0 = fma1(acc[j][8 * e + i], acc[j][8 * e + i], q0); q1 = fma1(acc[j][8 * e + i + 1], acc[j][8 * e + i + 1], q1); }
      n2[e] = q0 + q1;
    }
    auto sw2 = __builtin_amdgcn_permlane32_swap(__float_as_uint(n2[0]), __float_as_uint(n2[1]), false, false);
    const float kn = red32<true>(__uint_as_float(sw2[0]) + __uint_as_float(sw2[1]));
    __builtin_amdgcn_raw_buffer_store_b64(u32x2{__float_as_uint(kn), epoch}, sws, r == 0 ? (unsigned)(hh * 512) : OOB, WS_KN + (2 * (3 * cb + j) * 64 + tt) * 8, 16);
  }
}

constexpr int AT_TILE = 2080, AT_SLOT = 8 * AT_TILE, AT_EX = 3 * AT_SLOT;

#define EXP1(r) "v_exp_f32 v" #r ", v" #r "\n\t"
#define CVT1(d, a, b) "v_cvt_pk_bf16_f32 v" #d ", v" #a ", v" #b "\n\t"
#define MF(d, a, b, c) "v_mfma_f32_32x32x16_bf16 " d ", " a ", " b ", " c "\n\t"
#define R_OA "v[16:31]"
#define R_OB "v[32:47]"
#define R_SA "v[48:63]"
#define R_SB "v[64:79]"
#define R_C "v[80:95]"
#define R_QA "v[96:99]"
#define R_QB "v[100:103]"
#define R_K "v[104:107]"
#define R_V0 "v[108:111]"
#define R_V1 "v[112:115]"
#define R_P0 "v[116:119]"
#define R_P1 "v[120:123]"
#define PRHI "s_setprio 2\n\t"
#define PRLO "s_setprio 1\n\t"
#define XBAR "s_waitcnt vmcnt(0)\n\ts_barrier\n\t"
#define UNIT_A(PRE, KOFF, VOFF)                                                                                      \
  PRHI MF(R_SB, R_K, R_QB, R_C) EXP1(48) PRE "ds_read_b128 " R_K ", %[aK] offset:" KOFF "\n\t"                       \
  EXP1(49) EXP1(50) EXP1(51) EXP1(52) MF(R_OB, R_V1, R_P1, R_OB) EXP1(53)                                            \
  "ds_read_b128 " R_V1 ", %[aV1] offset:" VOFF "\n\t" EXP1(54) EXP1(55) EXP1(56)                                     \
  CVT1(116, 48, 49) CVT1(117, 50, 51) CVT1(118, 52, 53) CVT1(119, 54, 55) EXP1(57)                                   \
  "s_waitcnt lgkmcnt(2)\n\t" EXP1(58) PRLO MF(R_OA, R_V0, R_P0, R_OA) EXP1(59) EXP1(60) EXP1(61) EXP1(62)            \
  "s_waitcnt lgkmcnt(0)\n\t" EXP1(63)                                                                                \
  CVT1(120, 56, 57) CVT1(121, 58, 59) CVT1(122, 60, 61) CVT1(123, 62, 63)
#define UNIT_B(PRE, VOFF)                                                                                            \
  PRHI MF(R_SA, R_K, R_QA, R_C) EXP1(64) EXP1(65) EXP1(66) EXP1(67) EXP1(68) MF(R_OA, R_V1, R_P1, R_OA)              \
  EXP1(69) EXP1(70) EXP1(71) EXP1(72)                                                                                \
  CVT1(116, 64, 65) CVT1(117, 66, 67) CVT1(118, 68, 69) CVT1(119, 70, 71) EXP1(73) EXP1(74)                          \
  PRLO MF(R_OB, R_V0, R_P0, R_OB) EXP1(75) PRE "ds_read_b128 " R_V0 ", %[aV0] offset:" VOFF "\n\t"                   \
  EXP1(76) EXP1(77) EXP1(78) EXP1(79)                                                                                \
  CVT1(120, 72, 73) CVT1(121, 74, 75) CVT1(122, 76, 77) CVT1(123, 78, 79)

DI void attn_phase(char* ws, unsigned lds0, int b, int w, u64 tag) {
  const int lane = LANE_ID(), r = lane & 31, hh = lane >> 5, qp = w & 3, kh = w >> 2;
  const int head = (b & 7) * 4 + ((b >> 3) & 3), qb = b >> 5;
  if (w == 5 && lane < 48) {
    const unsigned i = lane, v = (i & 1) ? 0u : 0x3f803f80u;
    lds_st<u32x4>(lds0 + (i >> 1) * AT_TILE + 2048 + (i & 1) * 16, u32x4{v, v, v, v});
  }
  if (w == 4) {
    const int hp = head >> 1;
    const int f = lane < 16 ? ((16 + hp) / 3) * 16 + lane : lane < 32 ? ((32 + hp) / 3) * 16 + (lane - 16) : (hp / 3) * 16 + 2 * qb + (lane & 1);
    poll(ws, WS_FQ + f * FST, tag);
  }
  const srd_t skv = make_srd(ws + WS_KV + head * 131072, 131072), sws = make_srd(ws, WS_END);
  const unsigned voff = lane * 16, sbase = (unsigned)((32 * kh + qp) * 2048), mbase = lds0 + (unsigned)(w * AT_TILE);
  auto issue = [&](int c, int slot) {
    asm volatile("s_mov_b32 m0, %0\n\ts_nop 0\n\tbuffer_load_dwordx4 %1, %2, %3 offen sc1 lds\n\tbuffer_load_dwordx4 %1, %2, %3 offen offset:1024 sc1 lds"
                 ::"s"(mbase + (unsigned)(slot * AT_SLOT)), "v"(voff), "s"(skv), "s"(sbase + (unsigned)(c * 8192)) : "memory");
  };
  const int qt = qb * 8 + qp * 2;
  unsigned aK = lds0 + (unsigned)(kh * 4 * AT_TILE + lane * 16);
  const unsigned cst = (unsigned)(2048 + ((r == 16 || r == 20) ? 0 : 16));
  unsigned aV0 = lds0 + (unsigned)(kh * 4 * AT_TILE) + (r < 16 ? (unsigned)(1024 + hh * 256 + r * 16) : cst);
  unsigned aV1 = lds0 + (unsigned)(kh * 4 * AT_TILE) + (r < 16 ? (unsigned)(1536 + hh * 256 + r * 16) : cst);
  unsigned vkn = WS_KN + (32 + head) * 512 + lane * 8, vqn = WS_KN + head * 512 + qt * 8;
  f32x16 oa = {}, ob = {};
  asm volatile("" : "+v"(aK), "+v"(aV0), "+v"(aV1), "+v"(vkn), "+v"(vqn), "+{v[16:31]}"(oa), "+{v[32:47]}"(ob));
  lds_bar();
  __builtin_amdgcn_s_setprio(2);
  u32x4 qa, qbf, qn;
  u32x2 kn;
  asm volatile("buffer_load_dwordx2 %3, %5, %7, 0 offen sc1\n\tbuffer_load_dwordx4 %2, %6, %7, 0 offen sc1\n\t"
               "buffer_load_dwordx4 %0, %4, %7, %8 offen sc1\n\tbuffer_load_dwordx4 %1, %4, %7, %8 offen offset:1024 sc1"
               : "=&{v[96:99]}"(qa), "=&{v[100:103]}"(qbf), "=&v"(qn), "=&v"(kn) : "v"(voff), "v"(vkn), "v"(vqn), "s"(sws), "s"(WS_Q + (head * 64 + qt) * 1024) : "memory");
  issue(0, 0); issue(1, 1);
  asm volatile("s_waitcnt vmcnt(4)" : "+v"(qn), "+v"(kn), "+{v[96:99]}"(qa), "+{v[100:103]}"(qbf)::"memory");
  const unsigned ntag = (unsigned)tag;
  if (__builtin_expect(!all_lanes(kn[1] == ntag && qn[1] == ntag && qn[3] == ntag), 0)) {
#pragma unroll 1
    for (int it = 0; it < (1 << 16); ++it) {
      asm volatile("buffer_load_dwordx2 %1, %2, %4, 0 offen sc1\n\tbuffer_load_dwordx4 %0, %3, %4, 0 offen sc1\n\ts_waitcnt vmcnt(0)"
                   : "=&v"(qn), "=&v"(kn) : "v"(vkn), "v"(vqn), "s"(sws) : "memory");
      if (all_lanes(kn[1] == ntag && qn[1] == ntag && qn[3] == ntag)) break;
    }
  }
  const float bound = __builtin_amdgcn_sqrtf(comb<true>(__uint_as_float(qn[0]), __uint_as_float(qn[2])) * red_x32<true>(red32<true>(__uint_as_float(kn[0])))) * 1.01f;
  const bool safe = all_lanes(bound <= BOUND_LIMIT);
  f32x16 cneg;
#pragma unroll
  for (int i = 0; i < 16; ++i) cneg[i] = -bound;
  asm volatile("s_waitcnt vmcnt(2) lgkmcnt(0)\n\ts_barrier" : "+{v[96:99]}"(qa), "+{v[100:103]}"(qbf), "+{v[80:95]}"(cneg)::"memory");
  auto merge = [&](const f32x16& own, const f32x16& snd, float mo, float ms) {
    const unsigned ex = lds0 + AT_EX + w * 2560, ey = lds0 + AT_EX + (w ^ 4) * 2560;
    lds_st<f32x4>(ex + voff, f32x4{snd[0], snd[1], snd[2], snd[3]});
    lds_st<f32x4>(ex + 1024 + voff, f32x4{snd[4], snd[5], snd[6], snd[7]});
    lds_st<f32x2>(ex + 2048 + (voff >> 1), f32x2{snd[8], ms});
    lds_bar();
    const f32x2 t2 = lds_ld<f32x2>(ey + 2048 + (voff >> 1));
    const f32x4 t0 = lds_ld<f32x4>(ey + voff), t1 = lds_ld<f32x4>(ey + 1024 + voff);
    SBAR();
    const float mm = fmaxf(mo, t2[1]);
    const float f0 = __builtin_amdgcn_exp2f(mo - mm), f1 = __builtin_amdgcn_exp2f(t2[1] - mm);
    const float inv = __builtin_amdgcn_rcpf(__builtin_fmaf(own[8], f0, t2[0] * f1));
    float g0 = f0 * inv, g1 = f1 * inv, o[8];
    asm volatile("" : "+v"(g0), "+v"(g1));
#pragma unroll
    for (int i = 0; i < 8; ++i) o[i] = fma1(own[i], g0, mul1(i < 4 ? t0[i] : t1[i - 4], g1));
    st16c(sws, WS_O + (unsigned)(((qt + kh) * 32 + head) * 1024) + voff, u32x4{pk_bf16(o[0], o[1]), pk_bf16(o[2], o[3]), pk_bf16(o[4], o[5]), pk_bf16(o[6], o[7])});
  };
  if (__builtin_expect(safe, 1)) {
#define S0 "0"
#define S1 "16640"
#define S2 "33280"
#define CHUNK(S, SN, DMA)                                                                                            \
  UNIT_A("", S "+2080", S) UNIT_B("", S "+2080") UNIT_A("", S "+4160", S "+2080") UNIT_B("", S "+4160")              \
  UNIT_A("", S "+6240", S "+4160") UNIT_B("", S "+6240") UNIT_A(XBAR, SN, S "+6240") UNIT_B(DMA, SN)
#define KVDMA(MO, GO) "s_add_u32 m0, %[mbase], " MO "\n\ts_add_u32 %[t], %[sbase], " GO "\n\t"                       \
  "buffer_load_dwordx4 %[voff], %[skv], %[t] offen sc1 lds\n\tbuffer_load_dwordx4 %[voff], %[skv], %[t] offen offset:1024 sc1 lds\n\t"
#define HINT "s_cmp_lg_u32 %[w], 0\n\ts_cbranch_scc1 3f\n\tv_mov_b32 v116, %[tlo]\n\tv_mov_b32 v117, %[thi]\n\t"     \
  "buffer_store_dwordx2 v[116:117], off, %[sws], %[hoff] sc1\n\t3:\n\t"
    unsigned t;
    asm volatile(
        "ds_read_b128 " R_K ", %[aK]\n\t"
        "ds_read_b128 " R_V0 ", %[aV0]\n\t"
        "v_mov_b32 v112, 0\n\tv_mov_b32 v113, 0\n\tv_mov_b32 v114, 0\n\tv_mov_b32 v115, 0\n\t"
        "v_mov_b32 v120, 0\n\tv_mov_b32 v121, 0\n\tv_mov_b32 v122, 0\n\tv_mov_b32 v123, 0\n\t"
        "s_waitcnt lgkmcnt(0)\n\t" MF(R_SA, R_K, R_QA, R_C)
        "s_nop 11\n\t"
        CHUNK(S0, S1, KVDMA(S2, "16384")) CHUNK(S1, S2, KVDMA(S0, "24576")) CHUNK(S2, S0, KVDMA(S1, "32768"))
        CHUNK(S0, S1, KVDMA(S2, "40960")) CHUNK(S1, S2, KVDMA(S0, "49152")) CHUNK(S2, S0, KVDMA(S1, "57344"))
        CHUNK(S0, S1, "") HINT CHUNK(S1, S2, "")
        "s_nop 1\n\t" MF(R_OB, R_V1, R_P1, R_OB)
        "s_waitcnt vmcnt(0) lgkmcnt(0)\n\ts_nop 15\n\t"
        : "+{v[16:31]}"(oa), "+{v[32:47]}"(ob), [t] "=&s"(t)
        : "{v[80:95]}"(cneg), "{v[96:99]}"(qa), "{v[100:103]}"(qbf), [aK] "v"(aK), [aV0] "v"(aV0), [aV1] "v"(aV1), [voff] "v"(voff), [skv] "s"(skv),
          [mbase] "s"(mbase), [sbase] "s"(sbase), [w] "s"(w), [tlo] "s"((unsigned)tag), [thi] "s"((unsigned)(tag >> 32)), [sws] "s"(sws), [hoff] "s"(WS_FS + b * FST)
        : "memory", "scc", "v48", "v49", "v50", "v51", "v52", "v53", "v54", "v55", "v56", "v57", "v58", "v59", "v60", "v61", "v62", "v63", "v64",
          "v65", "v66", "v67", "v68", "v69", "v70", "v71", "v72", "v73", "v74", "v75", "v76", "v77", "v78", "v79", "v104", "v105", "v106",
          "v107", "v108", "v109", "v110", "v111", "v112", "v113", "v114", "v115", "v116", "v117", "v118", "v119", "v120", "v121", "v122", "v123");
    if (kh) merge(ob, oa, bound, bound); else merge(oa, ob, bound, bound);
  } else {
    if (w == 0 && lane == 0) flag_store(ws, WS_FS + b * FST, tag);
    float ma = -INFINITY, mb = -INFINITY;
    const s16x8 qf[2] = {__builtin_bit_cast(s16x8, qa), __builtin_bit_cast(s16x8, qbf)};
    int slot = 0;
#pragma unroll 1
    for (int c = 0; c < 8; ++c) {
#pragma unroll 1
      for (int jt = 0; jt < 4; ++jt) {
        const unsigned so = (unsigned)(slot * AT_SLOT + jt * AT_TILE);
        const s16x8 kf = lds_frag(aK + so), v0 = lds_frag(aV0 + so), v1 = lds_frag(aV1 + so);
#pragma unroll
        for (int qi = 0; qi < 2; ++qi) {
          f32x16& o = qi ? ob : oa;
          float& m = qi ? mb : ma;
          f32x16 s = MFMA(kf, qf[qi], f32x16{});
          float tm = s[0];
#pragma unroll
          for (int i = 1; i < 16; ++i) tm = fmaxf(tm, s[i]);
          auto sw2 = __builtin_amdgcn_permlane32_swap(__float_as_uint(tm), __float_as_uint(tm), false, false);
          const float mn = fmaxf(m, fmaxf(__uint_as_float(sw2[0]), __uint_as_float(sw2[1])));
          const float f = __builtin_amdgcn_exp2f(m - mn);
          m = mn;
#pragma unroll
          for (int i = 0; i < 16; ++i) { o[i] *= f; s[i] = __builtin_amdgcn_exp2f(s[i] - mn); }
          const u32x4 p0 = {pk_bf16(s[0], s[1]), pk_bf16(s[2], s[3]), pk_bf16(s[4], s[5]), pk_bf16(s[6], s[7])};
          const u32x4 p1 = {pk_bf16(s[8], s[9]), pk_bf16(s[10], s[11]), pk_bf16(s[12], s[13]), pk_bf16(s[14], s[15])};
          o = MFMA(v0, __builtin_bit_cast(s16x8, p0), o);
          o = MFMA(v1, __builtin_bit_cast(s16x8, p1), o);
        }
      }
      wait_bar<0>();
      if (c < 6) issue(c + 2, slot == 0 ? 2 : slot - 1);
      slot = slot == 2 ? 0 : slot + 1;
    }
    if (kh) merge(ob, oa, mb, ma); else merge(oa, ob, ma, mb);
  }
  asm volatile("s_waitcnt vmcnt(0)" ::: "memory");
  if (voff == 0) flag_store(ws, WS_FO + ((qt + kh) * 32 + head) * FST, tag);
}

struct Geglu {
  float a, d;
  DI Geglu(float d_) : a(__builtin_amdgcn_rcpf(d_)), d(d_) {}
  DI unsigned pair(f32x2 G, f32x2 U) const {
    const float c1 = -2.0f * 0.7978845608028654f * 1.4426950408889634f, c3 = c1 * 0.044715f;
    const f32x2 g = G * a, t = g * __builtin_elementwise_fma(g * g, f32x2{c3, c3}, f32x2{c1, c1});
    const f32x2 e = {__builtin_amdgcn_exp2f(t[0]), __builtin_amdgcn_exp2f(t[1])}, n = __builtin_elementwise_fma(e, f32x2{d, d}, f32x2{d, d});
    const f32x2 h = (g * U) * f32x2{__builtin_amdgcn_rcpf(n[0]), __builtin_amdgcn_rcpf(n[1])};
    return pk_bf16(h[0], h[1]);
  }
};

DI void up_tile(int j, int& tb, int& fb) {
  const int xcd = j & 7, i = j >> 3;
  const int u = (xcd & 1) * 28 + i;
  tb = 8 * (xcd >> 2) + (u & 7); fb = 7 * ((xcd >> 1) & 1) + (u >> 3);
}

struct UpDma {
  srd_t sx, sg, su;
  unsigned voX, voW, m0;
  DI UpDma(const char* x, const char* wg, const char* wu, unsigned lds0, int j, int w) {
    const int lane = LANE_ID();
    int tb, fb;
    up_tile(j, tb, fb);
    sx = make_srd(x, SEQ * DM * 2); sg = make_srd(wg, DM * DF * 2); su = make_srd(wu, DM * DF * 2);
    voX = (unsigned)((tb * 128 + 8 * w + (lane >> 3)) * 1024 + (((lane & 7) ^ ((4 * w + (lane >> 4)) & 7)) * 16));
    voW = (unsigned)((4 * w + (lane >> 4)) * (DF * 2) + fb * 256 + (((lane & 15) ^ (((lane >> 4) & 3) << 2)) * 16));
    m0 = lds0 + (unsigned)w * 1024u;
  }
  DI void X(int st) const { const unsigned k = st * 128; dma16(m0 + (st & 1) * 16384, voX, sx, k); dma16(m0 + (st & 1) * 16384 + 8192, voX, sx, k + 65536); }
  DI void W(int h) const { const unsigned k = h * (32 * DF * 2); dma16(m0 + 32768 + (h % 3) * 16384, voW, sg, k); dma16(m0 + 32768 + (h % 3) * 16384 + 8192, voW, su, k); }
};

DI void ln_phase(const char* x, char* ws, int j, int w, unsigned& epoch) {
  const int lane = LANE_ID();
  u32x4 xv;
  asm volatile("global_load_dwordx4 %0, %1, %2" : "=v"(xv) : "v"(lane * 16), "s"(x + (8 * j + w) * 1024) : "memory");
  epoch = get_epoch(ws);
  asm volatile("s_waitcnt vmcnt(0)" : "+v"(xv)::"memory");
  float f[8];
#pragma unroll
  for (int i = 0; i < 4; ++i) { f[2 * i] = bf_lo(xv[i]); f[2 * i + 1] = bf_hi(xv[i]); }
  const float mean = red_x32<false>(red32<false>(((f[0] + f[1]) + (f[2] + f[3])) + ((f[4] + f[5]) + (f[6] + f[7])))) * (1.0f / DM);
  float q = 0.f;
#pragma unroll
  for (int i = 0; i < 8; ++i) q = __builtin_fmaf(f[i] - mean, f[i] - mean, q);
  const float var = red_x32<false>(red32<false>(q)) * (1.0f / DM);
  const float m = rbf(mean), dd = rbf(__builtin_amdgcn_sqrtf(rbf(rbf(var) + 1e-5f)));
  if (lane == 0) flag_store(ws, WS_GS + (8 * j + w) * 8, ((u64)epoch << 32) | __float_as_uint(m) | (__float_as_uint(dd) >> 16));
  asm volatile("s_barrier" ::: "memory");
  if (w == 0 && lane == 0) flag_store(ws, WS_FL + j * FST, make_tag(epoch));
}

DI void up_phase(const char* x, const char* wg, const char* wu, char* ws, unsigned lds0, int j, int w, unsigned epoch) {
  const int lane = LANE_ID(), r = lane & 31, hh = lane >> 5, tp = w >> 2, ft = w & 3;
  int tb, fb;
  up_tile(j, tb, fb);
  const u64 tag = make_tag(epoch);
  const UpDma dma(x, wg, wu, lds0, j, w);
  if (w < 4) {
    for (int k = 0; k < w; ++k) __builtin_amdgcn_s_sleep(20);
    __builtin_amdgcn_s_setprio(3);
    touch(lds0 + 65536 + w * 256, (unsigned)((64 * (tb & 7) + 32 * (w >> 1) + (lane >> 1)) * (DF * 2) + fb * 256 + (lane & 1) * 128), make_srd((w & 1) ? wu : wg, DM * DF * 2), 0);
  }
  __builtin_amdgcn_s_setprio(0);
  dma.W(0); dma.X(0); dma.W(1);
  const int q = (lane & 15) >> 2, swz = (r >> 1) & 7;
  unsigned aX[4];
#pragma unroll
  for (int s = 0; s < 4; ++s) aX[s] = lds0 + (unsigned)((64 * tp + r) * 128 + (((2 * s + hh) ^ swz) * 16));
  const unsigned aG = lds0 + 32768 + (unsigned)((8 * hh + q) * 256 + ((ft ^ q) * 64) + ((lane >> 4) & 1) * 32 + (lane & 3) * 8);
  const srd_t sws = make_srd(ws, WS_END);
  f32x16 ga = {}, gb = {}, ua = {}, ub = {};
  s16x8 xa[2], xb[2];
  s16x4 gl[2], gh[2], ul[2], uh[2];
  u32x2 g0 = {}, g1 = {}, cg = {}, cu = {};
  auto consts = [&]() {
    g0 = __builtin_amdgcn_raw_buffer_load_b64(sws, r * 8, WS_GS + (tb * 128 + 64 * tp) * 8, 16);
    g1 = __builtin_amdgcn_raw_buffer_load_b64(sws, r * 8, WS_GS + (tb * 128 + 64 * tp + 32) * 8, 16);
    cg = __builtin_amdgcn_raw_buffer_load_b64(sws, r * 8, WS_GC + (fb * 128 + 32 * ft) * 8, 16);
    cu = __builtin_amdgcn_raw_buffer_load_b64(sws, r * 8, WS_GC + (DF + fb * 128 + 32 * ft) * 8, 16);
  };
#define TRD(A) __builtin_amdgcn_ds_read_tr16_b64_v4i16((LDSP s16x4*)(uintptr_t)(A))
#define CAT(L, H) __builtin_shufflevector(L, H, 0, 1, 2, 3, 4, 5, 6, 7)
#define URX(E, H) do { const unsigned a_ = aX[2 * ((H) & 1) + (E)] + (((H) >> 1) & 1) * 16384; xa[E] = lds_frag(a_); xb[E] = lds_frag(a_ + 4096); } while (0)
#define URG(E, H) do { const unsigned a_ = aG + (E) * 4096 + ((H) % 3) * 16384; gl[E] = TRD(a_); gh[E] = TRD(a_ + 1024); } while (0)
#define URU(E, H) do { const unsigned a_ = aG + (E) * 4096 + ((H) % 3) * 16384 + 8192; ul[E] = TRD(a_); uh[E] = TRD(a_ + 1024); } while (0)
#define UMM(P, N, H, RD) do { ga = MFMA(CAT(gl[P], gh[P]), xa[P], ga); SBAR(); if (RD) URX(N, H); SBAR(); gb = MFMA(CAT(gl[P], gh[P]), xb[P], gb); SBAR(); \
    if (RD) URG(N, H); SBAR(); ua = MFMA(CAT(ul[P], uh[P]), xa[P], ua); SBAR(); if (RD) URU(N, H); SBAR(); ub = MFMA(CAT(ul[P], uh[P]), xb[P], ub); SBAR(); } while (0)
  wait_bar<2>();
  URX(0, 0); URG(0, 0); URU(0, 0); SBAR();
  dma.W(2); dma.X(1);
#pragma unroll
  for (int h = 0; h < 16; ++h) {
    UMM(0, 1, h, true);
    if (h < 15) {
      if (h == 14) wait_bar<0>(); else if (h & 1) wait_bar<2>(); else wait_bar<4>();
    }
    UMM(1, 0, h + 1, h < 15);
    if (h + 3 < 16) dma.W(h + 3);
    if ((h & 1) && (h + 3) / 2 < 8) dma.X((h + 3) / 2);
    if (h == 12) consts();
  }
#undef UMM
#undef URU
#undef URG
#undef URX
#undef CAT
#undef TRD
  __builtin_amdgcn_s_setprio(3);
#pragma unroll 1
  for (int it = 0; it < (1 << 16) && !all_lanes(g0[1] == epoch && g1[1] == epoch && cg[1] == epoch && cu[1] == epoch); ++it) consts();
  {
    const float c = hh ? 0.f : __uint_as_float(cg[0]), c0 = rbf(c), c1 = rbf(c - c0), d = hh ? 0.f : __uint_as_float(cu[0]), d0 = rbf(d), d1 = rbf(d - d0);
    const s16x8 cf = __builtin_bit_cast(s16x8, u32x4{pk_bf16(c0, c1), pk_bf16((c - c0) - c1, 0.f), 0u, 0u});
    const s16x8 df = __builtin_bit_cast(s16x8, u32x4{pk_bf16(d0, d1), pk_bf16((d - d0) - d1, 0.f), 0u, 0u});
    const unsigned n0 = (g0[0] >> 16) ^ 0x8000u, n1 = (g1[0] >> 16) ^ 0x8000u;
    const s16x8 m0 = __builtin_bit_cast(s16x8, u32x4{n0 * 0x10001u, n0, 0u, 0u}), m1 = __builtin_bit_cast(s16x8, u32x4{n1 * 0x10001u, n1, 0u, 0u});
    ga = MFMA(cf, m0, ga); ua = MFMA(df, m0, ua); gb = MFMA(cf, m1, gb); ub = MFMA(df, m1, ub);
  }
  const unsigned ho = WS_H + (unsigned)(((tb * 4 + tp * 2) * 112 + fb * 8 + ft * 2) * 1024) + lane * 16;
#pragma unroll
  for (int t = 0; t < 2; ++t) {
    const f32x16& g = t ? gb : ga;
    const f32x16& u = t ? ub : ua;
    const Geglu act(bf_lo(t ? g1[0] : g0[0]));
#pragma unroll
    for (int e = 0; e < 2; ++e) {
      u32x4 pk;
#pragma unroll
      for (int k = 0; k < 4; ++k) pk[k] = act.pair(f32x2{g[8 * e + 2 * k], g[8 * e + 2 * k + 1]}, f32x2{u[8 * e + 2 * k], u[8 * e + 2 * k + 1]});
      st16c(sws, ho + t * 112 * 1024 + e * 1024, pk);
    }
  }
  publish(ws, WS_FH + (tb * 16 + fb) * FST, tag, w);
}

DI void colsum_phase(const char* wg, const char* wu, char* ws, unsigned lds0, int p, int w, unsigned epoch) {
  const int lane = LANE_ID(), hh = lane >> 5, q = (lane & 15) >> 2, ft = w & 3;
  const srd_t sw = make_srd(p < 14 ? wg : wu, DM * DF * 2);
  const unsigned voW = (unsigned)((4 * w + (lane >> 4)) * (DF * 2) + (p < 14 ? p : p - 14) * 256 + (((lane & 15) ^ (((lane >> 4) & 3) << 2)) * 16));
  const unsigned aW = lds0 + (unsigned)((128 * (w >> 2) + 8 * hh + q) * 256 + ((ft ^ q) * 64) + ((lane >> 4) & 1) * 32 + (lane & 3) * 8);
  const s16x4 ones = {0x3f80, 0x3f80, 0x3f80, 0x3f80};
  f32x4 cs = {};
#pragma unroll 1
  for (int half = 0; half < 2; ++half) {
#pragma unroll
    for (int k = 0; k < 8; ++k) dma16(lds0 + k * 8192 + w * 1024, voW, sw, (unsigned)((256 * half + 32 * k) * (DF * 2)));
    wait_bar<0>();
#pragma unroll
    for (int k = 0; k < 16; ++k)
      cs = __builtin_amdgcn_mfma_f32_4x4x4bf16_1k(ones, __builtin_amdgcn_ds_read_tr16_b64_v4i16((LDSP s16x4*)(uintptr_t)(aW + (k >> 1) * 4096 + (k & 1) * 1024)), cs, 0, 0, 0);
    lds_bar();
  }
  const float c = red_x32<false>(cs[0]);
  lds_st<float>(lds0 + w * 256 + lane * 4, c);
  lds_bar();
  if (w < 4 && lane < 32) flag_store(ws, WS_GC + (p * 128 + 32 * ft + lane) * 8, ((u64)epoch << 32) | __float_as_uint(c + lds_ld<float>(lds0 + (w + 4) * 256 + lane * 4)));
  lds_bar();
}

DI void out_phase(const char* x, const char* wd, const char* wo, char* ws, char* out, unsigned lds0, int j, int w, u64 tag) {
  __builtin_amdgcn_s_setprio(3);
  const int lane = LANE_ID(), r = lane & 31, hh = lane >> 5;
  const int xcd = j & 7, i = j >> 3, mb = 4 * xcd + (i & 3), nb = i >> 2;
  const int tl = w & 1, nt = (w >> 1) & 1, kh = w >> 2;
  const unsigned soH = (unsigned)(((2 * mb + (w >> 2)) * 112 + (w & 3)) * 1024);
  const int q = (lane & 15) >> 2;
  unsigned voX = lane * 16, voT = lane * 128, ovo = (unsigned)(r * 1024 + hh * 8), ovo2 = (unsigned)(r * 1024 + hh * 16);
  unsigned voW = (unsigned)((8 * w + (lane >> 3)) * 1024 + nb * 128 + (((lane & 7) ^ (((lane >> 4) & 1) << 2)) * 16));
  unsigned aX = lds0 + tl * 4096 + kh * 2048 + lane * 16;
  unsigned aW = lds0 + (unsigned)((32 * kh + 4 * hh + q) * 128 + ((nt ^ (q >> 1)) * 64) + ((lane >> 4) & 1) * 32 + (lane & 3) * 8);
  unsigned aX2 = aX + 32768, aW2 = aW + 32768;
  unsigned fho = WS_FH + ((mb >> 1) * 16 + (lane < 14 ? lane : 0)) * FST, foo = WS_FO + (2 * mb * 32 + lane) * FST;
  asm volatile("" : "+v"(voX), "+v"(voT), "+v"(ovo), "+v"(voW), "+v"(aX), "+v"(aW), "+v"(aX2), "+v"(aW2), "+v"(fho), "+v"(foo), "+v"(ovo2));
  __builtin_amdgcn_s_setprio(0);
  {
    const srd_t sw = make_srd(wo, DM * DM * 2);
#pragma unroll
    for (int st = 0; st < 8; ++st) dma16(lds0 + st * 8192 + w * 1024, voW, sw, st * 65536);
  }
  const unsigned oso = (unsigned)((64 * mb + 32 * tl) * 1024 + (64 * nb + 32 * nt + 16 * kh) * 2);
  const u32x2 xr0 = __builtin_amdgcn_raw_buffer_load_b64(make_srd(x, SEQ * DM * 2), ovo, oso, 0), xr1 = __builtin_amdgcn_raw_buffer_load_b64(make_srd(x, SEQ * DM * 2), ovo + 16, oso, 0);
  if (w < 7) touch(lds0 + 65536 + w * 256, voT, make_srd(wd, DF * DM * 2), (unsigned)((i * 7 + w) * 8192));
  unsigned seen = 0;
  auto need = [&](unsigned bits) {
    if (w == 0 && (seen & bits) != bits) {
#pragma unroll 1
      for (int it = 0; it < (1 << 16); ++it) {
        seen = (unsigned)__builtin_amdgcn_ballot_w64(__hip_atomic_load((gu64*)(ws + fho), __ATOMIC_RELAXED, __HIP_MEMORY_SCOPE_AGENT) == tag);
        if ((seen & bits) == bits) break;
        __builtin_amdgcn_s_sleep(4);
      }
    }
  };
  need(7u);
  wait_bar<0>();
  f32x16 acc;
  __builtin_amdgcn_s_setprio(3);
#pragma unroll
  for (int k = 0; k < 8; ++k) {
    const unsigned u = (k < 4 ? xr0 : xr1)[(k >> 1) & 1];
    const float xv = (k & 1) ? bf_hi(u) : bf_lo(u);
    acc[k] = kh ? 0.f : xv; acc[8 + k] = kh ? xv : 0.f;
  }
  asm volatile("" : "+v"(acc));
  __builtin_amdgcn_s_setprio(0);
  s16x8 wf[16];
#pragma unroll
  for (int k = 0; k < 16; ++k) wf[k] = lds_tr_frag(aW + (k >> 1) * 8192 + (k & 1) * 2048, aW + (k >> 1) * 8192 + (k & 1) * 2048 + 1024);
  lds_bar();
  const srd_t sh = make_srd(ws + WS_H, 64u * 112u * 1024u), sd = make_srd(wd, DF * DM * 2), so = make_srd(ws + WS_O, 2u << 20);
  const unsigned soO = (unsigned)(((2 * mb + (w >> 2)) * 32 + (w & 3)) * 1024);
  auto issue = [&](int st) {
    const unsigned sl = lds0 + (st % 5) * 16384 + (unsigned)w * 1024u;
    if (st < 28) { dma16c(sl, voX, sh, soH + st * 4096); dma16(sl + 8192, voW, sd, st * 65536); }
    else dma16c(sl, voX, so, soO + (st - 28) * 4096);
  };
  u64 oseen = 0;
  auto need_o = [&](u64 bits) {
    if (w == 0 && (oseen & bits) != bits) {
#pragma unroll 1
      for (int it = 0; it < (1 << 16); ++it) {
        oseen = __builtin_amdgcn_ballot_w64(__hip_atomic_load((gu64*)(ws + foo), __ATOMIC_RELAXED, __HIP_MEMORY_SCOPE_AGENT) == tag);
        if ((oseen & bits) == bits) break;
        __builtin_amdgcn_s_sleep(1);
      }
    }
  };
#pragma unroll
  for (int st = 0; st < 5; ++st) issue(st);
  {
    s16x8 x0, w0, x1, w1, x2, w2;
#define DRD(X, W, ST, E) do { const unsigned sl_ = ((ST) % 5) * 16384, ax_ = sl_ < 32768 ? aX + sl_ : aX2 + (sl_ - 32768), aw_ = sl_ < 32768 ? aW + sl_ : aW2 + (sl_ - 32768); \
    X = lds_frag(ax_ + (E) * 1024); if ((ST) < 28) W = lds_tr_frag(aw_ + 8192 + (E) * 2048, aw_ + 8192 + (E) * 2048 + 1024); else W = wf[2 * (((ST) - 28) & 7) + (E)]; SBAR(); } while (0)
    wait_bar<8>();
    DRD(x0, w0, 0, 0);
#pragma unroll
    for (int st = 0; st < 36; ++st) {
      if (st < 35) DRD(x1, w1, st, 1);
      acc = MFMA(w0, x0, acc); SBAR();
      if (st == 10 && w == 0) poll(ws, WS_FS + j * FST, tag);
      if (st + 5 >= 6 && st + 5 < 28 && ((st + 5) & 1) == 0) need(1u << ((st + 5) >> 1));
      if (st + 5 >= 28 && st + 5 < 36) need_o(0x0000000f0000000full << (4 * (st + 5 - 28)));
      if (st < 35) {
        const int n = (st + 2 < 36 ? (st + 2 < 28 ? 2 : 1) : 0) + (st + 3 < 36 ? (st + 3 < 28 ? 2 : 1) : 0) + (st + 4 < 36 ? (st + 4 < 28 ? 2 : 1) : 0);
        switch (n) { case 6: wait_bar<6>(); break; case 5: wait_bar<5>(); break; case 4: wait_bar<4>(); break; case 3: wait_bar<3>(); break;
                     case 2: wait_bar<2>(); break; case 1: wait_bar<1>(); break; default: wait_bar<0>(); }
        if (st + 5 < 36) issue(st + 5);
        DRD(x0, w0, st + 1, 0);
        if (st == 34) DRD(x2, w2, 35, 1);
        acc = MFMA(w1, x1, acc); SBAR();
      } else {
        acc = MFMA(w2, x2, acc); SBAR();
      }
    }
#undef DRD
  }
  auto finish = [&](const int keep) {
    const int o = 8 * (keep ^ 1), k0 = 8 * keep;
    const unsigned ex = lds0 + 32768 + voX;
    lds_st<f32x4>(ex + w * 2048, f32x4{acc[o], acc[o + 1], acc[o + 2], acc[o + 3]});
    lds_st<f32x4>(ex + w * 2048 + 1024, f32x4{acc[o + 4], acc[o + 5], acc[o + 6], acc[o + 7]});
    lds_bar();
    const f32x4 t0 = lds_ld<f32x4>(ex + (w ^ 4) * 2048), t1 = lds_ld<f32x4>(ex + (w ^ 4) * 2048 + 1024);
    const u32x2 o0 = {pk_bf16(acc[k0] + t0[0], acc[k0 + 1] + t0[1]), pk_bf16(acc[k0 + 2] + t0[2], acc[k0 + 3] + t0[3])};
    const u32x2 o1 = {pk_bf16(acc[k0 + 4] + t1[0], acc[k0 + 5] + t1[1]), pk_bf16(acc[k0 + 6] + t1[2], acc[k0 + 7] + t1[3])};
    auto s0 = __builtin_amdgcn_permlane32_swap(o0[0], o1[0], false, false), s1 = __builtin_amdgcn_permlane32_swap(o0[1], o1[1], false, false);
    __builtin_amdgcn_raw_buffer_store_b128(u32x4{s0[0], s1[0], s0[1], s1[1]}, make_srd(out, SEQ * DM * 2), ovo2, oso, 0);
  };
  if (kh) finish(1); else finish(0);
}

extern "C" __global__ __launch_bounds__(512, 4) void fused_block(const char* __restrict__ x, const char* __restrict__ wqkv, const char* __restrict__ wo,
                                                      const char* __restrict__ wg, const char* __restrict__ wu, const char* __restrict__ wd,
                                                      char* ws, char* __restrict__ out) {
  extern __shared__ __attribute__((aligned(16))) char lds[];
  const unsigned lds0 = (unsigned)(uintptr_t)(LDSP char*)lds;
  int w = __builtin_amdgcn_readfirstlane(TID >> 6);
  asm volatile("s_nop 4" : "+s"(w));
  unsigned epoch;
  asm volatile("s_mov_b32 %0, 0" : "=s"(epoch));
  const int b = BID & 255, rb = 8 * ((b & 7) >> 2) + ((b >> 3) & 7), cb = 4 * (b & 3) + (b >> 6);
  if (__builtin_expect(!(IS_A && w < 4), 1)) {
    if (__builtin_expect(IS_A, 1)) qkv_loader<0>(x, wqkv, ws, lds0, b, rb, cb, w, epoch);
    asm volatile("" ::: "memory");
    if (__builtin_expect(!IS_A, 1)) ln_phase(x, ws, b, w, epoch);
    asm volatile("" ::: "memory");
    if (__builtin_expect(IS_A, 1)) qkv_loader<1>(x, wqkv, ws, lds0, b, rb, cb, w, epoch);
  }
  asm volatile("" ::: "memory");
  if (__builtin_expect(IS_A && w < 4 && cb < 10, 1)) qkv_wave<0>(ws, lds0, rb, cb, w, epoch);
  asm volatile("" ::: "memory");
  if (__builtin_expect(IS_A && w < 4 && cb > 10, 1)) qkv_wave<3>(ws, lds0, rb, cb, w, epoch);
  asm volatile("" ::: "memory");
  if (__builtin_expect(IS_A && w < 4 && cb == 10, 1)) qkv_wave<1>(ws, lds0, rb, cb, w, epoch);
  asm volatile("" ::: "memory");
  if (__builtin_expect(IS_A, 1)) {
    const u64 tag = make_tag(epoch);
    attn_phase(ws, lds0, b, w, tag);
  } else {
    const int j = b;
    const u64 tag = make_tag(epoch);
    if (j < 224) {
      up_phase(x, wg, wu, ws, lds0, j, w, epoch);
    } else if (j < 252) {
      colsum_phase(wg, wu, ws, lds0, j - 224, w, epoch);
    }
    if (j == 255 && w == 1) {
      const int lane = LANE_ID();
#pragma unroll 1
      for (int k = 0; k < 8; ++k) poll(ws, WS_FQ + (k * 64 + lane) * FST, tag);
      if (lane == 0) __hip_atomic_store((gu32*)(ws + WS_EP), epoch, __ATOMIC_RELAXED, __HIP_MEMORY_SCOPE_AGENT);
    }
    out_phase(x, wd, wo, ws, out, lds0, j, w, tag);
  }
}

extern "C" void kernel_launch(void* const* d_in, const int* in_sizes, int n_in, void* d_out, int out_size, void* d_ws, size_t ws_size,
                              hipStream_t stream) {
  (void)hipFuncSetAttribute((const void*)fused_block, hipFuncAttributeMaxDynamicSharedMemorySize, LDS_BYTES);
  fused_block<<<512, 512, LDS_BYTES, stream>>>((const char*)d_in[0], (const char*)d_in[1], (const char*)d_in[2], (const char*)d_in[4],
                                               (const char*)d_in[5], (const char*)d_in[6], (char*)d_ws, (char*)d_out);
}
```
